```python
import torch

def _softmax(x, axis=-1):
    return torch.softmax(x, dim=axis)
def _gelu(x):
    return torch.nn.functional.gelu(x, approximate="tanh")
def _silu(x):
    return torch.nn.functional.silu(x)
def _sigmoid(x):
    return torch.sigmoid(x)
def _layernorm(x, axis=-1, eps=1e-5):
    m = x.mean(dim=axis, keepdim=True)
    v = x.var(dim=axis, keepdim=True, unbiased=False)
    return (x - m) / torch.sqrt(v + eps)
def _scatter_add(tgt, idx, src, axis=0):
    return tgt.clone().index_add_(axis, idx, src)
def _segment_sum(src, seg, n):
    out = torch.zeros((n,) + src.shape[1:], dtype=src.dtype)
    return out.index_add_(0, seg, src)
def _pad(x, pw):
    flat = tuple(v for pair in reversed(pw) for v in pair)
    return torch.nn.functional.pad(x, flat)

kernel_name = 'concat_heads__ssm_conv_h3__ssm_conv_h3__prompt'

def setup_inputs():

    _w = (lambda a: (lambda t: t.to(torch.bfloat16) if t.is_floating_point() else t)(torch.as_tensor(a)))
    def _A_setup():
        B, S, D = 1, 2048, 640
        ks = 4
        return {
            "q": _w((lambda *s: torch.randn(*s, dtype=torch.bfloat16) * 0.05)(B, S, D)),
            "k": _w((lambda *s: torch.randn(*s, dtype=torch.bfloat16) * 0.05)(B, S, D)),
            "v": _w((lambda *s: torch.randn(*s, dtype=torch.bfloat16) * 0.05)(B, S, D)),
            "shift_k": _w((lambda *s: torch.randn(*s, dtype=torch.bfloat16) * 0.05)(D, ks) * 0.5),
            "ssm_k": _w((lambda *s: torch.randn(*s, dtype=torch.bfloat16) * 0.05)(D, S) * (1.0 / S ** 0.5)),
            "ssm_D": _w((lambda *s: torch.randn(*s, dtype=torch.bfloat16) * 0.05)(D)),
        }
    _A_in = _A_setup()

    def _B_setup():
        B, S, D = 1, 2048, 640
        ks = 4
        return {
            "q": _w((lambda *s: torch.randn(*s, dtype=torch.bfloat16) * 0.05)(B, S, D)),
            "k": _w((lambda *s: torch.randn(*s, dtype=torch.bfloat16) * 0.05)(B, S, D)),
            "v": _w((lambda *s: torch.randn(*s, dtype=torch.bfloat16) * 0.05)(B, S, D)),
            "shift_k": _w((lambda *s: torch.randn(*s, dtype=torch.bfloat16) * 0.05)(D, ks) * 0.5),
            "ssm_k": _w((lambda *s: torch.randn(*s, dtype=torch.bfloat16) * 0.05)(D, S) * (1.0 / S ** 0.5)),
            "ssm_D": _w((lambda *s: torch.randn(*s, dtype=torch.bfloat16) * 0.05)(D)),
        }
    _B_in = _B_setup()

    return {
        "A_q": _A_in["q"],
        "A_k": _A_in["k"],
        "A_v": _A_in["v"],
        "A_shift_k": _A_in["shift_k"],
        "A_ssm_k": _A_in["ssm_k"],
        "A_ssm_D": _A_in["ssm_D"],
        "B_q": _B_in["q"],
        "B_k": _B_in["k"],
        "B_v": _B_in["v"],
        "B_shift_k": _B_in["shift_k"],
        "B_ssm_k": _B_in["ssm_k"],
        "B_ssm_D": _B_in["ssm_D"],
    }

def reference(A_q, A_k, A_v, A_shift_k, A_ssm_k, A_ssm_D, B_q, B_k, B_v, B_shift_k, B_ssm_k, B_ssm_D):
    def _A_ref(q, k, v, shift_k, ssm_k, ssm_D):
        B, S, D = q.shape
        ks = shift_k.shape[-1]
        k_pad = _pad(k, ((0, 0), (ks - 1, 0), (0, 0)))
        k_shift = torch.zeros_like(k)
        for t in range(ks):
            k_shift = k_shift + k_pad[:, t:t + S, :] * shift_k[:, ks - 1 - t][None, None, :]
        kv = k_shift * v
        L = 2 * S
        kv_pad = _pad(kv, ((0, 0), (0, S), (0, 0)))
        kf_pad = _pad(ssm_k, ((0, 0), (0, S)))
        kv_f = (lambda x, axes=(-2,-1): torch.fft.rfft2(x.float(), dim=axes))(kv_pad[:, None, :, :], axes=(1, 2))
        kf_f = (lambda x, axes=(-2,-1): torch.fft.rfft2(x.float(), dim=axes))(kf_pad[:, None, :], axes=(1, 2))
        kf_f = (lambda x, *d: x.permute(*d))(kf_f, 1, 2, 0)
        y_f = kv_f * kf_f[None, :, :, :]
        y = (lambda x, s, axes=(-2,-1): torch.fft.irfft2(x, s=s, dim=axes).to(torch.bfloat16))(y_f, (1, L), axes=(1, 2))[:, 0, :S, :]
        y = y + kv * ssm_D[None, None, :]
        return q * y

    def _B_ref(q, k, v, shift_k, ssm_k, ssm_D):
        B, S, D = q.shape
        ks = shift_k.shape[-1]
        k_pad = _pad(k, ((0, 0), (ks - 1, 0), (0, 0)))
        k_shift = torch.zeros_like(k)
        for t in range(ks):
            k_shift = k_shift + k_pad[:, t:t + S, :] * shift_k[:, ks - 1 - t][None, None, :]
        kv = k_shift * v
        L = 2 * S
        kv_pad = _pad(kv, ((0, 0), (0, S), (0, 0)))
        kf_pad = _pad(ssm_k, ((0, 0), (0, S)))
        kv_f = (lambda x, axes=(-2,-1): torch.fft.rfft2(x.float(), dim=axes))(kv_pad[:, None, :, :], axes=(1, 2))
        kf_f = (lambda x, axes=(-2,-1): torch.fft.rfft2(x.float(), dim=axes))(kf_pad[:, None, :], axes=(1, 2))
        kf_f = (lambda x, *d: x.permute(*d))(kf_f, 1, 2, 0)
        y_f = kv_f * kf_f[None, :, :, :]
        y = (lambda x, s, axes=(-2,-1): torch.fft.irfft2(x, s=s, dim=axes).to(torch.bfloat16))(y_f, (1, L), axes=(1, 2))[:, 0, :S, :]
        y = y + kv * ssm_D[None, None, :]
        return q * y

    a = _A_ref(A_q, A_k, A_v, A_shift_k, A_ssm_k, A_ssm_D)
    b = _B_ref(B_q, B_k, B_v, B_shift_k, B_ssm_k, B_ssm_D)
    return torch.concatenate([a, b], axis=-1)
```

```cpp
#include <hip/hip_runtime.h>
#include <hip/hip_bf16.h>
#include <stdint.h>

#define EXACT 1

typedef unsigned short u16;
__device__ __forceinline__ float bf2f(u16 x) { return __uint_as_float(((unsigned)x) << 16); }
__device__ __forceinline__ u16 f2bf(float f) { __hip_bfloat16 b = __float2bfloat16(f); return *(u16*)&b; }
__device__ __forceinline__ float rb(float f) { return bf2f(f2bf(f)); }

constexpr int S = 2048, D = 640;

__global__ void k1(const u16* kA, const u16* vA, const u16* skA, const u16* kB, const u16* vB, const u16* skB, u16* kvT) {
  int idx = blockIdx.x * blockDim.x + threadIdx.x;
  int ch = idx % 1280, s = idx / 1280;
  const u16* k = ch < D ? kA : kB; const u16* v = ch < D ? vA : vB; const u16* sk = ch < D ? skA : skB;
  int d = ch < D ? ch : ch - D;
  float acc = 0.f;
  for (int t = 0; t < 4; ++t) {
    int r = s - 3 + t;
    float kk = r >= 0 ? bf2f(k[r * D + d]) : 0.f;
    float w = bf2f(sk[d * 4 + 3 - t]);
    acc = rb(acc + rb(kk * w));
  }
  float kv = acc * bf2f(v[s * D + d]);
  kvT[ch * S + s] = f2bf(kv);
}

__global__ void k2(const u16* qA, const u16* hA, const u16* DA, const u16* qB, const u16* hB, const u16* DB, const u16* kvT, u16* out) {
  __shared__ float xs[S]; __shared__ float hs[S];
  int ch = blockIdx.x;
  const u16* q = ch < D ? qA : qB; const u16* h = ch < D ? hA : hB; const u16* Dd = ch < D ? DA : DB;
  int d = ch < D ? ch : ch - D;
  for (int i = threadIdx.x; i < S; i += blockDim.x) { xs[i] = bf2f(kvT[ch * S + i]); hs[i] = bf2f(h[d * S + i]); }
  __syncthreads();
  float dd = bf2f(Dd[d]);
  for (int n = threadIdx.x; n < S; n += blockDim.x) {
    float acc = 0.f;
    for (int j = 0; j <= n; ++j) acc = fmaf(hs[j], xs[n - j], acc);
    float y = rb(rb(acc) + rb(xs[n] * dd));
    out[n * 1280 + ch] = f2bf(bf2f(q[n * D + d]) * y);
  }
}

extern "C" void kernel_launch(void* const* d_in, const int* in_sizes, int n_in,
                              void* d_out, int out_size, void* d_ws, size_t ws_size,
                              hipStream_t stream) {
  const u16* const* in = (const u16* const*)d_in;
  u16* kvT = (u16*)d_ws;
  k1<<<S * 1280 / 256, 256, 0, stream>>>(in[1], in[2], in[3], in[7], in[8], in[9], kvT);
  k2<<<1280, 256, 0, stream>>>(in[0], in[4], in[5], in[6], in[10], in[11], kvT, (u16*)d_out);
}
```

```cpp
#include <hip/hip_runtime.h>
#include <stdint.h>

typedef unsigned short u16;
typedef unsigned long long u64;
typedef __attribute__((ext_vector_type(4))) unsigned u32x4;
typedef __attribute__((ext_vector_type(2))) unsigned u32x2;
typedef __attribute__((ext_vector_type(2))) float f32x2;
typedef __attribute__((ext_vector_type(4))) float f32x4;
typedef __attribute__((ext_vector_type(16))) float f32x16;
typedef __attribute__((ext_vector_type(2))) __bf16 bf16x2;
typedef __attribute__((ext_vector_type(4))) __bf16 bf16x4;
typedef __attribute__((ext_vector_type(8))) __bf16 bf16x8;
#define LAS __attribute__((address_space(3)))
#define GAS __attribute__((address_space(1)))

constexpr int TBYTES = 2048 * 1280;
constexpr int HSTR = 4160;
constexpr int XOFF = 8 * HSTR;
constexpr int XSTR = 4544;
constexpr int YOFF = XOFF + 8 * XSTR + 32;
constexpr int YSTR = 8224;
constexpr int FOFF = YOFF + 8 * YSTR;
constexpr int LDS_BYTES = FOFF + 64;
constexpr int NHELP = 12;

#define stamp(i) do {} while (0)

__device__ __forceinline__ unsigned pk(float a, float b) {
  bf16x2 p = __builtin_convertvector(f32x2{a, b}, bf16x2);
  return __builtin_bit_cast(unsigned, p);
}
__device__ __forceinline__ float lo16(unsigned d) { return __uint_as_float(d << 16); }
__device__ __forceinline__ float hi16(unsigned d) { return __uint_as_float(d & 0xffff0000u); }
__device__ __forceinline__ f32x2 unpk(unsigned d) { return f32x2{lo16(d), hi16(d)}; }
__device__ __forceinline__ float ror1(float v) {
  return __builtin_bit_cast(float, __builtin_amdgcn_update_dpp(0, __builtin_bit_cast(int, v), 0x13C, 0xf, 0xf, false));
}
__device__ __forceinline__ unsigned lds_ld(LAS char* p) {
  return __builtin_amdgcn_readfirstlane(__hip_atomic_load((LAS unsigned*)p, __ATOMIC_RELAXED, __HIP_MEMORY_SCOPE_WORKGROUP));
}
__device__ __forceinline__ bf16x8 ld_h(LAS char* p) {
  bf16x4 a = __builtin_amdgcn_ds_read_tr16_b64_v4bf16((LAS bf16x4*)(p + 32));
  bf16x4 b = __builtin_amdgcn_ds_read_tr16_b64_v4bf16((LAS bf16x4*)(p));
  return __builtin_shufflevector(a, b, 0, 1, 2, 3, 4, 5, 6, 7);
}

__global__ __launch_bounds__(1024, 4) void h3_kernel(
    const u16* __restrict__ qA, const u16* __restrict__ kA, const u16* __restrict__ vA, const u16* __restrict__ sA,
    const u16* __restrict__ hA, const u16* __restrict__ dA,
    const u16* __restrict__ qB, const u16* __restrict__ kB, const u16* __restrict__ vB, const u16* __restrict__ sB,
    const u16* __restrict__ hB, const u16* __restrict__ dB, u16* __restrict__ out
    ) {
  extern __shared__ __attribute__((aligned(16))) char smem_[];
  LAS char* smem = (LAS char*)smem_;
  const int wv = __builtin_amdgcn_readfirstlane(threadIdx.x >> 6), lane = threadIdx.x & 63;
  const int b = blockIdx.x, xg = b & 7, ci = b >> 3;
  const bool half = xg >= 4;
  stamp(0);
  const auto rk = __builtin_amdgcn_make_buffer_rsrc((void*)(half ? kB : kA), 0, TBYTES, 0x00020000);
  const auto rv = __builtin_amdgcn_make_buffer_rsrc((void*)(half ? vB : vA), 0, TBYTES, 0x00020000);
  const auto rq = __builtin_amdgcn_make_buffer_rsrc((void*)(half ? qB : qA), 0, TBYTES, 0x00020000);

  if (ci >= 20) {
    const int hw = (ci - 20) * 16 + wv;
    const int lr = lane / 20, lc = lane - lr * 20;
    const unsigned col = (xg & 3) * 320 + lc * 16;
    u32x4 acc = {0, 0, 0, 0};
    u32x4 t[12];
#pragma unroll
    for (int i = 0; i < 4; ++i) {
      const int row = (i * 192 + hw) * 3 + lr;
      const unsigned off = (lane < 60 && row < 2048) ? row * 1280 + col : 0x80000000u;
      t[2 * i] = __builtin_amdgcn_raw_buffer_load_b128(rk, off, 0, 0);
      t[2 * i + 1] = __builtin_amdgcn_raw_buffer_load_b128(rv, off, 0, 0);
    }
#pragma unroll
    for (int i = 0; i < 4; ++i) {
      const int row = (i * 192 + hw) * 3 + lr;
      const unsigned off = (lane < 60 && row < 2048) ? row * 1280 + col : 0x80000000u;
      t[8 + i] = __builtin_amdgcn_raw_buffer_load_b128(rq, off, 0, 0);
    }
#pragma unroll
    for (int i = 0; i < 12; ++i) asm volatile("" :: "v"(t[i]));
    stamp(1);
    return;
  }

  const int ol = (xg & 3) * 20 + ci;
  LAS char* flags = smem + FOFF;

  if (wv >= 8) {
    const int dl = wv - 8;
    const int j = lane >> 1, hq = lane & 1;
    const int row0 = dl * 256 + j * 8;
    const unsigned voff = row0 * 1280 + ol * 16 + hq * 8;
    const unsigned hoff = ((lane & 15) < 2 && row0 > 0) ? voff - 3840 : 0x80000000u;
    u32x2 kk[11], vv[8];
    if (dl > 0) {
      __builtin_amdgcn_s_barrier();
      while (lds_ld(flags + 4) < (unsigned)dl) __builtin_amdgcn_s_sleep(1);
    }
    asm volatile("" ::: "memory");
#pragma unroll
    for (int i = 0; i < 3; ++i) kk[i] = __builtin_amdgcn_raw_buffer_load_b64(rk, hoff + i * 1280, 0, 0);
#pragma unroll
    for (int i = 0; i < 8; ++i) kk[3 + i] = __builtin_amdgcn_raw_buffer_load_b64(rk, voff + i * 1280, 0, 0);
#pragma unroll
    for (int i = 0; i < 6; ++i) vv[i] = __builtin_amdgcn_raw_buffer_load_b64(rv, voff + i * 1280, 0, 0);
    __builtin_amdgcn_sched_barrier(0);
    if (dl == 0) __builtin_amdgcn_s_barrier();
    __hip_atomic_store((LAS unsigned*)(flags + 4), (unsigned)dl + 1, __ATOMIC_RELAXED, __HIP_MEMORY_SCOPE_WORKGROUP);
    __builtin_amdgcn_sched_barrier(0);
#pragma unroll
    for (int i = 6; i < 8; ++i) vv[i] = __builtin_amdgcn_raw_buffer_load_b64(rv, voff + i * 1280, 0, 0);
    const int ch0 = ol * 8 + hq * 4;
    const u32x4* skp = (const u32x4*)((half ? sB : sA) + ch0 * 4);
    const u32x4 wr0 = skp[0], wr1 = skp[1];
    const u32x2 dr = *(const u32x2*)((half ? dB : dA) + ch0);
    __builtin_amdgcn_sched_barrier(0);
    stamp(1);
#pragma unroll
    for (int i = 0; i < 3; ++i) {
      kk[i].x = __builtin_amdgcn_update_dpp(kk[i].x, kk[8 + i].x, 0x112, 0xf, 0xf, false);
      kk[i].y = __builtin_amdgcn_update_dpp(kk[i].y, kk[8 + i].y, 0x112, 0xf, 0xf, false);
    }
    f32x2 e[2][8];
    LAS char* xw = smem + XOFF + 448 + hq * (4 * XSTR + 32) + (dl * 8 + (j >> 2)) * 64 + (j & 3) * 4;
#pragma unroll
    for (int jj = 0; jj < 2; ++jj) {
      const u32x4 wr = jj ? wr1 : wr0;
      const f32x2 w0 = {lo16(wr.x), lo16(wr.z)}, w1 = {hi16(wr.x), hi16(wr.z)};
      const f32x2 w2 = {lo16(wr.y), lo16(wr.w)}, w3 = {hi16(wr.y), hi16(wr.w)};
      f32x2 kf[11], x[8];
#pragma unroll
      for (int i = 0; i < 11; ++i) kf[i] = unpk(kk[i][jj]);
#pragma unroll
      for (int r = 0; r < 8; ++r) {
        f32x2 a = kf[r] * w3;
        a = __builtin_elementwise_fma(kf[r + 1], w2, a);
        a = __builtin_elementwise_fma(kf[r + 2], w1, a);
        a = __builtin_elementwise_fma(kf[r + 3], w0, a);
        x[r] = a * unpk(vv[r][jj]);
      }
#pragma unroll
      for (int s = 0; s < 4; ++s) {
        *(LAS unsigned*)(xw + (2 * jj) * XSTR + s * 16) = pk(x[s].x, x[s + 4].x);
        *(LAS unsigned*)(xw + (2 * jj + 1) * XSTR + s * 16) = pk(x[s].y, x[s + 4].y);
      }
#pragma unroll
      for (int r = 0; r < 8; ++r) e[jj][r] = x[r];
    }
    asm volatile("s_waitcnt lgkmcnt(0)" ::: "memory");
    __hip_atomic_fetch_or((LAS unsigned*)flags, 1u << dl, __ATOMIC_RELAXED, __HIP_MEMORY_SCOPE_WORKGROUP);
    stamp(2);
    while (lds_ld(flags + 4) < (unsigned)(8 + dl)) __builtin_amdgcn_s_sleep(1);
    asm volatile("" ::: "memory");
    u32x2 qq[8];
#pragma unroll
    for (int i = 0; i < 6; ++i) qq[i] = __builtin_amdgcn_raw_buffer_load_b64(rq, voff + i * 1280, 0, 0);
    __builtin_amdgcn_sched_barrier(0);
    __hip_atomic_store((LAS unsigned*)(flags + 4), (unsigned)dl + 9, __ATOMIC_RELAXED, __HIP_MEMORY_SCOPE_WORKGROUP);
    __builtin_amdgcn_sched_barrier(0);
#pragma unroll
    for (int i = 6; i < 8; ++i) qq[i] = __builtin_amdgcn_raw_buffer_load_b64(rq, voff + i * 1280, 0, 0);
    __builtin_amdgcn_sched_barrier(0);
    f32x2 qf[2][8];
#pragma unroll
    for (int jj = 0; jj < 2; ++jj) {
      const f32x2 dd = unpk(dr[jj]);
#pragma unroll
      for (int r = 0; r < 8; ++r) { qf[jj][r] = unpk(qq[r][jj]); e[jj][r] = e[jj][r] * dd * qf[jj][r]; }
    }
#pragma unroll
    for (int jj = 0; jj < 2; ++jj)
#pragma unroll
      for (int r = 0; r < 8; ++r) asm volatile("" : "+v"(e[jj][r]), "+v"(qf[jj][r]));
    stamp(3);
    while (lds_ld(flags + 8 + 4 * dl) < 8u) __builtin_amdgcn_s_sleep(1);
    asm volatile("" ::: "memory");
    stamp(4);
    LAS char* yr = smem + YOFF + dl * YSTR + hq * (4096 + 16) + j * 32;
    f32x4 y[4][2];
#pragma unroll
    for (int cc = 0; cc < 4; ++cc) { y[cc][0] = *(LAS f32x4*)(yr + cc * 1024); y[cc][1] = *(LAS f32x4*)(yr + cc * 1024 + 16); }
    const auto ro = __builtin_amdgcn_make_buffer_rsrc((void*)out, 0, 2 * TBYTES, 0x00020000);
    const unsigned ooff = row0 * 2560 + (half ? 1280 : 0) + ol * 16 + hq * 8;
#pragma unroll
    for (int r = 0; r < 8; ++r) {
      const float o0 = fmaf(qf[0][r].x, y[0][r >> 2][r & 3], e[0][r].x);
      const float o1 = fmaf(qf[0][r].y, y[1][r >> 2][r & 3], e[0][r].y);
      const float o2 = fmaf(qf[1][r].x, y[2][r >> 2][r & 3], e[1][r].x);
      const float o3 = fmaf(qf[1][r].y, y[3][r >> 2][r & 3], e[1][r].y);
      __builtin_amdgcn_raw_buffer_store_b64(u32x2{pk(o0, o1), pk(o2, o3)}, ro, ooff + r * 2560, 0, 0);
    }
  } else {
    const int c = wv;
    const u32x4* hp = (const u32x4*)((half ? hB : hA) + (size_t)(ol * 8 + c) * 2048) + lane;
    u32x4 hv[4];
#pragma unroll
    for (int i = 0; i < 4; ++i) hv[i] = hp[i * 64];
    __builtin_amdgcn_sched_barrier(0);
    LAS char* hb = smem + c * HSTR;
    LAS char* xb = smem + XOFF + c * XSTR + (c >> 2) * 32;
    if (lane < 28) *(LAS u32x4*)(xb + lane * 16) = u32x4{0, 0, 0, 0};
    else if (lane < 32) *(LAS u32x4*)(hb + (lane - 28) * 16) = u32x4{0, 0, 0, 0};
    else if (lane < 36 && c == 0) *(LAS u32x4*)(flags + (lane - 32) * 16) = u32x4{0, 0, 0, 0};
    asm volatile("s_waitcnt lgkmcnt(0)" ::: "memory");
    __builtin_amdgcn_s_barrier();
#pragma unroll
    for (int i = 0; i < 4; ++i) *(LAS u32x4*)(hb + 64 + i * 1024 + lane * 16) = hv[i];
    stamp(1);
    LAS char* hl = hb + 64 - 32 + 2 * (16 * (lane >> 4) + 4 * ((lane & 3) - ((lane >> 2) & 3)));
    LAS char* xl = xb + 448 - 384 - 64 * (lane >> 5) + (lane & 31) * 16;
    LAS char* yw = smem + YOFF + c * 1024 + (c >> 2) * 16 + lane * 4;
    const bool l63 = lane == 63;
    float c0 = 0.f, c1 = 0.f, c2 = 0.f;
    unsigned xm = 0;
    auto waitx = [&](int d) {
      if (!((xm >> d) & 1)) {
        while (!(((xm = lds_ld(flags)) >> d) & 1)) __builtin_amdgcn_s_sleep(1);
        asm volatile("" ::: "memory");
      }
    };
    auto finish = [&](const f32x16& a, int t) {
#pragma unroll
      for (int i = 0; i < 4; ++i) {
        const float t0 = a[4 * i + 3];
        const float t1 = a[4 * i + 2] + ror1(l63 ? c0 : t0);
        const float t2 = a[4 * i + 1] + ror1(l63 ? c1 : t1);
        const float yv = a[4 * i + 0] + ror1(l63 ? c2 : t2);
        c0 = t0; c1 = t1; c2 = t2;
        *(LAS float*)(yw + t * YSTR + i * 256) = yv;
      }
      asm volatile("s_waitcnt lgkmcnt(0)" ::: "memory");
      if (lane == 0) __hip_atomic_fetch_add((LAS unsigned*)(flags + 8 + 4 * t), 1u, __ATOMIC_RELAXED, __HIP_MEMORY_SCOPE_WORKGROUP);
    };
#pragma unroll 1
    for (int t0 = 0; t0 < 8; t0 += 2) {
      f32x16 a0 = {}, a1 = {};
      bf16x8 HA[4], HB[4];
#pragma unroll
      for (int r = 0; r < 4; ++r) HA[r] = ld_h(hl + (t0 + 1) * 512 + r * 128);
      auto step = [&](int d, const bf16x8(&P)[4], bf16x8(&N)[4]) {
        waitx(d);
        LAS char* xp = xl + d * 512;
        LAS char* hq_ = hl + (t0 - d) * 512;
        bf16x8 X[4];
#pragma unroll
        for (int r = 0; r < 4; ++r) { X[r] = *(LAS bf16x8*)(xp + (3 - r) * 128); N[r] = ld_h(hq_ + r * 128); }
#pragma unroll
        for (int r = 0; r < 4; ++r) {
          a1 = __builtin_amdgcn_mfma_f32_32x32x16_bf16(X[r], P[r], a1, 0, 0, 0);
          a0 = __builtin_amdgcn_mfma_f32_32x32x16_bf16(X[r], N[r], a0, 0, 0, 0);
        }
      };
#pragma unroll 1
      for (int d = 0;; d += 2) {
        step(d, HA, HB);
        if (d == t0) break;
        step(d + 1, HB, HA);
      }
      if (t0 == 0) stamp(2);
      waitx(t0 + 1);
      {
        LAS char* xp = xl + (t0 + 1) * 512;
        bf16x8 X[4];
#pragma unroll
        for (int r = 0; r < 4; ++r) X[r] = *(LAS bf16x8*)(xp + (3 - r) * 128);
#pragma unroll
        for (int r = 0; r < 4; ++r) a1 = __builtin_amdgcn_mfma_f32_32x32x16_bf16(X[r], HB[r], a1, 0, 0, 0);
      }
      finish(a0, t0);
      finish(a1, t0 + 1);
    }
  }
}

extern "C" void kernel_launch(void* const* d_in, const int* in_sizes, int n_in, void* d_out, int out_size,
                              void* d_ws, size_t ws_size, hipStream_t stream) {
  const u16* const* in = (const u16* const*)d_in;
  hipFuncSetAttribute((const void*)h3_kernel, hipFuncAttributeMaxDynamicSharedMemorySize, LDS_BYTES);
  h3_kernel<<<8 * (20 + NHELP), 1024, LDS_BYTES, stream>>>(in[0], in[1], in[2], in[3], in[4], in[5], in[6], in[7], in[8],
                                                          in[9], in[10], in[11], (u16*)d_out);
}
```

```cpp
#include <hip/hip_runtime.h>
#include <stdint.h>

typedef unsigned short u16;
typedef unsigned long long u64;
typedef __attribute__((ext_vector_type(4))) unsigned u32x4;
typedef __attribute__((ext_vector_type(2))) unsigned u32x2;
typedef __attribute__((ext_vector_type(2))) float f32x2;
typedef __attribute__((ext_vector_type(4))) float f32x4;
typedef __attribute__((ext_vector_type(16))) float f32x16;
typedef __attribute__((ext_vector_type(2))) __bf16 bf16x2;
typedef __attribute__((ext_vector_type(4))) __bf16 bf16x4;
typedef __attribute__((ext_vector_type(8))) __bf16 bf16x8;
#define LAS __attribute__((address_space(3)))
#define GAS __attribute__((address_space(1)))

constexpr int TBYTES = 2048 * 1280;
constexpr int HSTR = 4160;
constexpr int XOFF = 8 * HSTR;
constexpr int XSTR = 4544;
constexpr int YOFF = XOFF + 8 * XSTR + 32;
constexpr int YSTR = 8224;
constexpr int FOFF = YOFF + 8 * YSTR;
constexpr int LDS_BYTES = FOFF + 64;
constexpr int NHELP = 12;

#define stamp(i) do {} while (0)

__device__ __forceinline__ unsigned pk(float a, float b) {
  bf16x2 p = __builtin_convertvector(f32x2{a, b}, bf16x2);
  return __builtin_bit_cast(unsigned, p);
}
__device__ __forceinline__ float lo16(unsigned d) { return __uint_as_float(d << 16); }
__device__ __forceinline__ float hi16(unsigned d) { return __uint_as_float(d & 0xffff0000u); }
__device__ __forceinline__ f32x2 unpk(unsigned d) { return f32x2{lo16(d), hi16(d)}; }
__device__ __forceinline__ float ror1(float v) {
  return __builtin_bit_cast(float, __builtin_amdgcn_update_dpp(0, __builtin_bit_cast(int, v), 0x13C, 0xf, 0xf, false));
}
__device__ __forceinline__ unsigned lds_ld(LAS char* p) {
  return __builtin_amdgcn_readfirstlane(__hip_atomic_load((LAS unsigned*)p, __ATOMIC_RELAXED, __HIP_MEMORY_SCOPE_WORKGROUP));
}
__device__ __forceinline__ bf16x8 ld_h(LAS char* p) {
  bf16x4 a = __builtin_amdgcn_ds_read_tr16_b64_v4bf16((LAS bf16x4*)(p + 32));
  bf16x4 b = __builtin_amdgcn_ds_read_tr16_b64_v4bf16((LAS bf16x4*)(p));
  return __builtin_shufflevector(a, b, 0, 1, 2, 3, 4, 5, 6, 7);
}

__global__ __launch_bounds__(1024, 4) void h3_kernel(
    const u16* __restrict__ qA, const u16* __restrict__ kA, const u16* __restrict__ vA, const u16* __restrict__ sA,
    const u16* __restrict__ hA, const u16* __restrict__ dA,
    const u16* __restrict__ qB, const u16* __restrict__ kB, const u16* __restrict__ vB, const u16* __restrict__ sB,
    const u16* __restrict__ hB, const u16* __restrict__ dB, u16* __restrict__ out
    ) {
  extern __shared__ __attribute__((aligned(16))) char smem_[];
  LAS char* smem = (LAS char*)smem_;
  const int wv = __builtin_amdgcn_readfirstlane(threadIdx.x >> 6), lane = threadIdx.x & 63;
  const int b = blockIdx.x, xg = b & 7, ci = b >> 3;
  const bool half = xg >= 4;
  stamp(0);
  const auto rk = __builtin_amdgcn_make_buffer_rsrc((void*)(half ? kB : kA), 0, TBYTES, 0x00020000);
  const auto rv = __builtin_amdgcn_make_buffer_rsrc((void*)(half ? vB : vA), 0, TBYTES, 0x00020000);
  const auto rq = __builtin_amdgcn_make_buffer_rsrc((void*)(half ? qB : qA), 0, TBYTES, 0x00020000);

  if (ci >= 20) {
    const int hw = (ci - 20) * 16 + wv;
    const int lr = lane / 20, lc = lane - lr * 20;
    const unsigned col = (xg & 3) * 320 + lc * 16;
    u32x4 acc = {0, 0, 0, 0};
    u32x4 t[12];
#pragma unroll
    for (int i = 0; i < 4; ++i) {
      const int row = (i * 192 + hw) * 3 + lr;
      const unsigned off = (lane < 60 && row < 2048) ? row * 1280 + col : 0x80000000u;
      t[2 * i] = __builtin_amdgcn_raw_buffer_load_b128(rk, off, 0, 0);
      t[2 * i + 1] = __builtin_amdgcn_raw_buffer_load_b128(rv, off, 0, 0);
    }
#pragma unroll
    for (int i = 0; i < 4; ++i) {
      const int row = (i * 192 + hw) * 3 + lr;
      const unsigned off = (lane < 60 && row < 2048) ? row * 1280 + col : 0x80000000u;
      t[8 + i] = __builtin_amdgcn_raw_buffer_load_b128(rq, off, 0, 0);
    }
#pragma unroll
    for (int i = 0; i < 12; ++i) asm volatile("" :: "v"(t[i]));
    stamp(1);
    return;
  }

  const int ol = (xg & 3) * 20 + ci;
  LAS char* flags = smem + FOFF;

  if (wv >= 8) {
    const int dl = wv - 8;
    const int j = lane >> 1, hq = lane & 1;
    const int row0 = dl * 256 + j * 8;
    const unsigned voff = row0 * 1280 + ol * 16 + hq * 8;
    const unsigned hoff = ((lane & 15) < 2 && row0 > 0) ? voff - 3840 : 0x80000000u;
    u32x2 kk[11], vv[8];
    if (dl > 0) {
      __builtin_amdgcn_s_barrier();
      while (lds_ld(flags + 4) < (unsigned)dl) __builtin_amdgcn_s_sleep(1);
    }
    asm volatile("" ::: "memory");
#pragma unroll
    for (int i = 0; i < 3; ++i) kk[i] = __builtin_amdgcn_raw_buffer_load_b64(rk, hoff + i * 1280, 0, 0);
#pragma unroll
    for (int i = 0; i < 8; ++i) kk[3 + i] = __builtin_amdgcn_raw_buffer_load_b64(rk, voff + i * 1280, 0, 0);
#pragma unroll
    for (int i = 0; i < 6; ++i) vv[i] = __builtin_amdgcn_raw_buffer_load_b64(rv, voff + i * 1280, 0, 0);
    __builtin_amdgcn_sched_barrier(0);
    if (dl == 0) __builtin_amdgcn_s_barrier();
    __hip_atomic_store((LAS unsigned*)(flags + 4), (unsigned)dl + 1, __ATOMIC_RELAXED, __HIP_MEMORY_SCOPE_WORKGROUP);
    __builtin_amdgcn_sched_barrier(0);
#pragma unroll
    for (int i = 6; i < 8; ++i) vv[i] = __builtin_amdgcn_raw_buffer_load_b64(rv, voff + i * 1280, 0, 0);
    const int ch0 = ol * 8 + hq * 4;
    const u32x4* skp = (const u32x4*)((half ? sB : sA) + ch0 * 4);
    const u32x4 wr0 = skp[0], wr1 = skp[1];
    const u32x2 dr = *(const u32x2*)((half ? dB : dA) + ch0);
    __builtin_amdgcn_sched_barrier(0);
    stamp(1);
#pragma unroll
    for (int i = 0; i < 3; ++i) {
      kk[i].x = __builtin_amdgcn_update_dpp(kk[i].x, kk[8 + i].x, 0x112, 0xf, 0xf, false);
      kk[i].y = __builtin_amdgcn_update_dpp(kk[i].y, kk[8 + i].y, 0x112, 0xf, 0xf, false);
    }
    f32x2 e[2][8];
    LAS char* xw = smem + XOFF + 448 + hq * (4 * XSTR + 32) + (dl * 8 + (j >> 2)) * 64 + (j & 3) * 4;
#pragma unroll
    for (int jj = 0; jj < 2; ++jj) {
      const u32x4 wr = jj ? wr1 : wr0;
      const f32x2 w0 = {lo16(wr.x), lo16(wr.z)}, w1 = {hi16(wr.x), hi16(wr.z)};
      const f32x2 w2 = {lo16(wr.y), lo16(wr.w)}, w3 = {hi16(wr.y), hi16(wr.w)};
      f32x2 kf[11], x[8];
#pragma unroll
      for (int i = 0; i < 11; ++i) kf[i] = unpk(kk[i][jj]);
#pragma unroll
      for (int r = 0; r < 8; ++r) {
        f32x2 a = kf[r] * w3;
        a = __builtin_elementwise_fma(kf[r + 1], w2, a);
        a = __builtin_elementwise_fma(kf[r + 2], w1, a);
        a = __builtin_elementwise_fma(kf[r + 3], w0, a);
        x[r] = a * unpk(vv[r][jj]);
      }
#pragma unroll
      for (int s = 0; s < 4; ++s) {
        *(LAS unsigned*)(xw + (2 * jj) * XSTR + s * 16) = pk(x[s].x, x[s + 4].x);
        *(LAS unsigned*)(xw + (2 * jj + 1) * XSTR + s * 16) = pk(x[s].y, x[s + 4].y);
      }
#pragma unroll
      for (int r = 0; r < 8; ++r) e[jj][r] = x[r];
    }
    asm volatile("s_waitcnt lgkmcnt(0)" ::: "memory");
    __hip_atomic_fetch_or((LAS unsigned*)flags, 1u << dl, __ATOMIC_RELAXED, __HIP_MEMORY_SCOPE_WORKGROUP);
    stamp(2);
    while (lds_ld(flags + 4) < (unsigned)(8 + dl)) __builtin_amdgcn_s_sleep(1);
    asm volatile("" ::: "memory");
    u32x2 qq[8];
#pragma unroll
    for (int i = 0; i < 6; ++i) qq[i] = __builtin_amdgcn_raw_buffer_load_b64(rq, voff + i * 1280, 0, 0);
    __builtin_amdgcn_sched_barrier(0);
    __hip_atomic_store((LAS unsigned*)(flags + 4), (unsigned)dl + 9, __ATOMIC_RELAXED, __HIP_MEMORY_SCOPE_WORKGROUP);
    __builtin_amdgcn_sched_barrier(0);
#pragma unroll
    for (int i = 6; i < 8; ++i) qq[i] = __builtin_amdgcn_raw_buffer_load_b64(rq, voff + i * 1280, 0, 0);
    __builtin_amdgcn_sched_barrier(0);
    f32x2 qf[2][8];
#pragma unroll
    for (int jj = 0; jj < 2; ++jj) {
      const f32x2 dd = unpk(dr[jj]);
#pragma unroll
      for (int r = 0; r < 8; ++r) { qf[jj][r] = unpk(qq[r][jj]); e[jj][r] = e[jj][r] * dd * qf[jj][r]; }
    }
#pragma unroll
    for (int jj = 0; jj < 2; ++jj)
#pragma unroll
      for (int r = 0; r < 8; ++r) asm volatile("" : "+v"(e[jj][r]), "+v"(qf[jj][r]));
    stamp(3);
    while (lds_ld(flags + 8 + 4 * dl) < 8u) __builtin_amdgcn_s_sleep(1);
    asm volatile("" ::: "memory");
    stamp(4);
    LAS char* yr = smem + YOFF + dl * YSTR + hq * (4096 + 16) + j * 32;
    f32x4 y[4][2];
#pragma unroll
    for (int cc = 0; cc < 4; ++cc) { y[cc][0] = *(LAS f32x4*)(yr + cc * 1024); y[cc][1] = *(LAS f32x4*)(yr + cc * 1024 + 16); }
    const auto ro = __builtin_amdgcn_make_buffer_rsrc((void*)out, 0, 2 * TBYTES, 0x00020000);
    const unsigned ooff = row0 * 2560 + (half ? 1280 : 0) + ol * 16 + hq * 8;
#pragma unroll
    for (int r = 0; r < 8; ++r) {
      const float o0 = fmaf(qf[0][r].x, y[0][r >> 2][r & 3], e[0][r].x);
      const float o1 = fmaf(qf[0][r].y, y[1][r >> 2][r & 3], e[0][r].y);
      const float o2 = fmaf(qf[1][r].x, y[2][r >> 2][r & 3], e[1][r].x);
      const float o3 = fmaf(qf[1][r].y, y[3][r >> 2][r & 3], e[1][r].y);
      __builtin_amdgcn_raw_buffer_store_b64(u32x2{pk(o0, o1), pk(o2, o3)}, ro, ooff + r * 2560, 0, 0);
    }
  } else {
    const int c = wv;
    const u32x4* hp = (const u32x4*)((half ? hB : hA) + (size_t)(ol * 8 + c) * 2048) + lane;
    u32x4 hv[4];
#pragma unroll
    for (int i = 0; i < 4; ++i) hv[i] = hp[i * 64];
    __builtin_amdgcn_sched_barrier(0);
    LAS char* hb = smem + c * HSTR;
    LAS char* xb = smem + XOFF + c * XSTR + (c >> 2) * 32;
    if (lane < 28) *(LAS u32x4*)(xb + lane * 16) = u32x4{0, 0, 0, 0};
    else if (lane < 32) *(LAS u32x4*)(hb + (lane - 28) * 16) = u32x4{0, 0, 0, 0};
    else if (lane < 36 && c == 0) *(LAS u32x4*)(flags + (lane - 32) * 16) = u32x4{0, 0, 0, 0};
    asm volatile("s_waitcnt lgkmcnt(0)" ::: "memory");
    __builtin_amdgcn_s_barrier();
#pragma unroll
    for (int i = 0; i < 4; ++i) *(LAS u32x4*)(hb + 64 + i * 1024 + lane * 16) = hv[i];
    stamp(1);
    LAS char* hl = hb + 64 - 32 + 2 * (16 * (lane >> 4) + 4 * ((lane & 3) - ((lane >> 2) & 3)));
    LAS char* xl = xb + 448 - 384 - 64 * (lane >> 5) + (lane & 31) * 16;
    LAS char* yw = smem + YOFF + c * 1024 + (c >> 2) * 16 + lane * 4;
    const bool l63 = lane == 63;
    float c0 = 0.f, c1 = 0.f, c2 = 0.f;
    unsigned xm = 0;
    auto waitx = [&](int d) {
      if (!((xm >> d) & 1)) {
        while (!(((xm = lds_ld(flags)) >> d) & 1)) __builtin_amdgcn_s_sleep(1);
        asm volatile("" ::: "memory");
      }
    };
    auto finish = [&](const f32x16& a, int t) {
#pragma unroll
      for (int i = 0; i < 4; ++i) {
        const float t0 = a[4 * i + 3];
        const float t1 = a[4 * i + 2] + ror1(l63 ? c0 : t0);
        const float t2 = a[4 * i + 1] + ror1(l63 ? c1 : t1);
        const float yv = a[4 * i + 0] + ror1(l63 ? c2 : t2);
        c0 = t0; c1 = t1; c2 = t2;
        *(LAS float*)(yw + t * YSTR + i * 256) = yv;
      }
      asm volatile("s_waitcnt lgkmcnt(0)" ::: "memory");
      if (lane == 0) __hip_atomic_fetch_add((LAS unsigned*)(flags + 8 + 4 * t), 1u, __ATOMIC_RELAXED, __HIP_MEMORY_SCOPE_WORKGROUP);
    };
#pragma unroll 1
    for (int t0 = 0; t0 < 8; t0 += 2) {
      f32x16 a0 = {}, a1 = {};
      bf16x8 HA[4], HB[4];
#pragma unroll
      for (int r = 0; r < 4; ++r) HA[r] = ld_h(hl + (t0 + 1) * 512 + r * 128);
      auto step = [&](int d, const bf16x8(&P)[4], bf16x8(&N)[4]) {
        waitx(d);
        LAS char* xp = xl + d * 512;
        LAS char* hq_ = hl + (t0 - d) * 512;
        bf16x8 X[4];
#pragma unroll
        for (int r = 0; r < 4; ++r) { X[r] = *(LAS bf16x8*)(xp + (3 - r) * 128); N[r] = ld_h(hq_ + r * 128); }
#pragma unroll
        for (int r = 0; r < 4; ++r) {
          a1 = __builtin_amdgcn_mfma_f32_32x32x16_bf16(X[r], P[r], a1, 0, 0, 0);
          a0 = __builtin_amdgcn_mfma_f32_32x32x16_bf16(X[r], N[r], a0, 0, 0, 0);
        }
      };
#pragma unroll 1
      for (int d = 0;; d += 2) {
        step(d, HA, HB);
        if (d == t0) break;
        step(d + 1, HB, HA);
      }
      if (t0 == 0) stamp(2);
      waitx(t0 + 1);
      {
        LAS char* xp = xl + (t0 + 1) * 512;
        bf16x8 X[4];
#pragma unroll
        for (int r = 0; r < 4; ++r) X[r] = *(LAS bf16x8*)(xp + (3 - r) * 128);
#pragma unroll
        for (int r = 0; r < 4; ++r) a1 = __builtin_amdgcn_mfma_f32_32x32x16_bf16(X[r], HB[r], a1, 0, 0, 0);
      }
      finish(a0, t0);
      finish(a1, t0 + 1);
    }
  }
}

struct P8 { const u16* p[8]; };
__global__ __launch_bounds__(256) void prek(P8 a, int has_out, u16* sink) {
  int tid = blockIdx.x * 256 + threadIdx.x;
  u32x4 r[20];
#pragma unroll
  for (int i = 0; i < 20; ++i) { int c = tid + i * 65536; int t = c / 163840; int cc = c - t * 163840; r[i] = ((const u32x4*)a.p[t])[cc]; }
#pragma unroll
  for (int i = 0; i < 20; ++i) asm volatile("" :: "v"(r[i]));
}
template <int STRIDE>
__global__ __launch_bounds__(256) void touchk(P8 a, const u16* out) {
  int t = (blockIdx.x >> 3) * 256 + threadIdx.x;
  int n = TBYTES / STRIDE + 1;
  unsigned x = 0;
  for (int i = t; i < n * 10; i += (gridDim.x >> 3) * 256) {
    int bi = i / n, pg = i - bi * n; size_t off = (size_t)pg * STRIDE; if (off >= TBYTES) off = TBYTES - 4;
    const char* base = bi < 8 ? (const char*)a.p[bi] : (const char*)out + (bi - 8) * (size_t)TBYTES;
    x ^= *(const unsigned*)(base + off);
  }
  asm volatile("" :: "v"(x));
}
extern "C" void kernel_launch(void* const* d_in, const int* in_sizes, int n_in, void* d_out, int out_size,
                              void* d_ws, size_t ws_size, hipStream_t stream) {
  const u16* const* in = (const u16* const*)d_in;
  hipFuncSetAttribute((const void*)h3_kernel, hipFuncAttributeMaxDynamicSharedMemorySize, LDS_BYTES);
  P8 a; a.p[0]=in[0]; a.p[1]=in[1]; a.p[2]=in[2]; a.p[3]=in[4]; a.p[4]=in[6]; a.p[5]=in[7]; a.p[6]=in[8]; a.p[7]=in[10];
  touchk<1048576><<<8, 256, 0, stream>>>(a, (const u16*)d_out);
  h3_kernel<<<8 * (20 + NHELP), 1024, LDS_BYTES, stream>>>(in[0], in[1], in[2], in[3], in[4], in[5], in[6], in[7], in[8],
                                                          in[9], in[10], in[11], (u16*)d_out);
}
```

```cpp
#define DIAGW 0
#define DIAGS 0
#include <hip/hip_runtime.h>
#include <stdint.h>

typedef unsigned short u16;
typedef unsigned long long u64;
typedef __attribute__((ext_vector_type(4))) unsigned u32x4;
typedef __attribute__((ext_vector_type(2))) unsigned u32x2;
typedef __attribute__((ext_vector_type(2))) float f32x2;
typedef __attribute__((ext_vector_type(4))) float f32x4;
typedef __attribute__((ext_vector_type(16))) float f32x16;
typedef __attribute__((ext_vector_type(2))) __bf16 bf16x2;
typedef __attribute__((ext_vector_type(4))) __bf16 bf16x4;
typedef __attribute__((ext_vector_type(8))) __bf16 bf16x8;
#define LAS __attribute__((address_space(3)))
#define GAS __attribute__((address_space(1)))

constexpr int TBYTES = 2048 * 1280;
constexpr int HSTR = 4160;
constexpr int XOFF = 8 * HSTR;
constexpr int XSTR = 4544;
constexpr int YOFF = XOFF + 8 * XSTR + 32;
constexpr int YSTR = 8224;
constexpr int FOFF = YOFF + 8 * YSTR;
constexpr int LDS_BYTES = FOFF + 64;
constexpr int NHELP = 12;

#define stamp(i) do { __builtin_amdgcn_sched_barrier(0); asm volatile("s_memrealtime %0\n\ts_waitcnt lgkmcnt(0)" : "=s"(T[i]) :: "memory"); __builtin_amdgcn_sched_barrier(0); } while (0)

__device__ __forceinline__ unsigned pk(float a, float b) {
  bf16x2 p = __builtin_convertvector(f32x2{a, b}, bf16x2);
  return __builtin_bit_cast(unsigned, p);
}
__device__ __forceinline__ float lo16(unsigned d) { return __uint_as_float(d << 16); }
__device__ __forceinline__ float hi16(unsigned d) { return __uint_as_float(d & 0xffff0000u); }
__device__ __forceinline__ f32x2 unpk(unsigned d) { return f32x2{lo16(d), hi16(d)}; }
__device__ __forceinline__ float ror1(float v) {
  return __builtin_bit_cast(float, __builtin_amdgcn_update_dpp(0, __builtin_bit_cast(int, v), 0x13C, 0xf, 0xf, false));
}
__device__ __forceinline__ unsigned lds_ld(LAS char* p) {
  return __builtin_amdgcn_readfirstlane(__hip_atomic_load((LAS unsigned*)p, __ATOMIC_RELAXED, __HIP_MEMORY_SCOPE_WORKGROUP));
}
__device__ __forceinline__ bf16x8 ld_h(LAS char* p) {
  bf16x4 a = __builtin_amdgcn_ds_read_tr16_b64_v4bf16((LAS bf16x4*)(p + 32));
  bf16x4 b = __builtin_amdgcn_ds_read_tr16_b64_v4bf16((LAS bf16x4*)(p));
  return __builtin_shufflevector(a, b, 0, 1, 2, 3, 4, 5, 6, 7);
}

__global__ __launch_bounds__(1024, 4) void h3_kernel(
    const u16* __restrict__ qA, const u16* __restrict__ kA, const u16* __restrict__ vA, const u16* __restrict__ sA,
    const u16* __restrict__ hA, const u16* __restrict__ dA,
    const u16* __restrict__ qB, const u16* __restrict__ kB, const u16* __restrict__ vB, const u16* __restrict__ sB,
    const u16* __restrict__ hB, const u16* __restrict__ dB, u16* __restrict__ out
    ) {
  extern __shared__ __attribute__((aligned(16))) char smem_[];
  LAS char* smem = (LAS char*)smem_;
  const int wv = __builtin_amdgcn_readfirstlane(threadIdx.x >> 6), lane = threadIdx.x & 63;
  const int b = blockIdx.x, xg = b & 7, ci = b >> 3;
  const bool half = xg >= 4;
  u64 T[8] = {0, 0, 0, 0, 0, 0, 0, 0};
  stamp(0);
  const auto rk = __builtin_amdgcn_make_buffer_rsrc((void*)(half ? kB : kA), 0, TBYTES, 0x00020000);
  const auto rv = __builtin_amdgcn_make_buffer_rsrc((void*)(half ? vB : vA), 0, TBYTES, 0x00020000);
  const auto rq = __builtin_amdgcn_make_buffer_rsrc((void*)(half ? qB : qA), 0, TBYTES, 0x00020000);

  if (ci >= 20) {
    const int hw = (ci - 20) * 16 + wv;
    const int lr = lane / 20, lc = lane - lr * 20;
    const unsigned col = (xg & 3) * 320 + lc * 16;
    u32x4 acc = {0, 0, 0, 0};
    u32x4 t[12];
#pragma unroll
    for (int i = 0; i < 4; ++i) {
      const int row = (i * 192 + hw) * 3 + lr;
      const unsigned off = (lane < 60 && row < 2048) ? row * 1280 + col : 0x80000000u;
      t[2 * i] = __builtin_amdgcn_raw_buffer_load_b128(rk, off, 0, 0);
      t[2 * i + 1] = __builtin_amdgcn_raw_buffer_load_b128(rv, off, 0, 0);
    }
#pragma unroll
    for (int i = 0; i < 4; ++i) {
      const int row = (i * 192 + hw) * 3 + lr;
      const unsigned off = (lane < 60 && row < 2048) ? row * 1280 + col : 0x80000000u;
      t[8 + i] = __builtin_amdgcn_raw_buffer_load_b128(rq, off, 0, 0);
    }
#pragma unroll
    for (int i = 0; i < 12; ++i) asm volatile("" :: "v"(t[i]));
    stamp(1);
    return;
  }

  const int ol = (xg & 3) * 20 + ci;
  LAS char* flags = smem + FOFF;

  if (wv >= 8) {
    const int dl = wv - 8;
    const int j = lane >> 1, hq = lane & 1;
    const int row0 = dl * 256 + j * 8;
    const unsigned voff = row0 * 1280 + ol * 16 + hq * 8;
    const unsigned hoff = ((lane & 15) < 2 && row0 > 0) ? voff - 3840 : 0x80000000u;
    u32x2 kk[11], vv[8];
    if (dl > 0) {
      __builtin_amdgcn_s_barrier();
      while (lds_ld(flags + 4) < (unsigned)dl) __builtin_amdgcn_s_sleep(1);
    }
    asm volatile("" ::: "memory");
#pragma unroll
    for (int i = 0; i < 3; ++i) kk[i] = __builtin_amdgcn_raw_buffer_load_b64(rk, hoff + i * 1280, 0, 0);
#pragma unroll
    for (int i = 0; i < 8; ++i) kk[3 + i] = __builtin_amdgcn_raw_buffer_load_b64(rk, voff + i * 1280, 0, 0);
#pragma unroll
    for (int i = 0; i < 6; ++i) vv[i] = __builtin_amdgcn_raw_buffer_load_b64(rv, voff + i * 1280, 0, 0);
    __builtin_amdgcn_sched_barrier(0);
    if (dl == 0) __builtin_amdgcn_s_barrier();
    __hip_atomic_store((LAS unsigned*)(flags + 4), (unsigned)dl + 1, __ATOMIC_RELAXED, __HIP_MEMORY_SCOPE_WORKGROUP);
    __builtin_amdgcn_sched_barrier(0);
#pragma unroll
    for (int i = 6; i < 8; ++i) vv[i] = __builtin_amdgcn_raw_buffer_load_b64(rv, voff + i * 1280, 0, 0);
    const int ch0 = ol * 8 + hq * 4;
    const u32x4* skp = (const u32x4*)((half ? sB : sA) + ch0 * 4);
    const u32x4 wr0 = skp[0], wr1 = skp[1];
    const u32x2 dr = *(const u32x2*)((half ? dB : dA) + ch0);
    __builtin_amdgcn_sched_barrier(0);
    stamp(1);
#pragma unroll
    for (int i = 0; i < 3; ++i) {
      kk[i].x = __builtin_amdgcn_update_dpp(kk[i].x, kk[8 + i].x, 0x112, 0xf, 0xf, false);
      kk[i].y = __builtin_amdgcn_update_dpp(kk[i].y, kk[8 + i].y, 0x112, 0xf, 0xf, false);
    }
    f32x2 e[2][8];
    LAS char* xw = smem + XOFF + 448 + hq * (4 * XSTR + 32) + (dl * 8 + (j >> 2)) * 64 + (j & 3) * 4;
#pragma unroll
    for (int jj = 0; jj < 2; ++jj) {
      const u32x4 wr = jj ? wr1 : wr0;
      const f32x2 w0 = {lo16(wr.x), lo16(wr.z)}, w1 = {hi16(wr.x), hi16(wr.z)};
      const f32x2 w2 = {lo16(wr.y), lo16(wr.w)}, w3 = {hi16(wr.y), hi16(wr.w)};
      f32x2 kf[11], x[8];
#pragma unroll
      for (int i = 0; i < 11; ++i) kf[i] = unpk(kk[i][jj]);
#pragma unroll
      for (int r = 0; r < 8; ++r) {
        f32x2 a = kf[r] * w3;
        a = __builtin_elementwise_fma(kf[r + 1], w2, a);
        a = __builtin_elementwise_fma(kf[r + 2], w1, a);
        a = __builtin_elementwise_fma(kf[r + 3], w0, a);
        x[r] = a * unpk(vv[r][jj]);
      }
#pragma unroll
      for (int s = 0; s < 4; ++s) {
        *(LAS unsigned*)(xw + (2 * jj) * XSTR + s * 16) = pk(x[s].x, x[s + 4].x);
        *(LAS unsigned*)(xw + (2 * jj + 1) * XSTR + s * 16) = pk(x[s].y, x[s + 4].y);
      }
#pragma unroll
      for (int r = 0; r < 8; ++r) e[jj][r] = x[r];
    }
    asm volatile("s_waitcnt lgkmcnt(0)" ::: "memory");
    __hip_atomic_fetch_or((LAS unsigned*)flags, 1u << dl, __ATOMIC_RELAXED, __HIP_MEMORY_SCOPE_WORKGROUP);
    stamp(2);
    while (lds_ld(flags + 4) < (unsigned)(8 + dl)) __builtin_amdgcn_s_sleep(1);
    asm volatile("" ::: "memory");
    u32x2 qq[8];
#pragma unroll
    for (int i = 0; i < 6; ++i) qq[i] = __builtin_amdgcn_raw_buffer_load_b64(rq, voff + i * 1280, 0, 0);
    __builtin_amdgcn_sched_barrier(0);
    __hip_atomic_store((LAS unsigned*)(flags + 4), (unsigned)dl + 9, __ATOMIC_RELAXED, __HIP_MEMORY_SCOPE_WORKGROUP);
    __builtin_amdgcn_sched_barrier(0);
#pragma unroll
    for (int i = 6; i < 8; ++i) qq[i] = __builtin_amdgcn_raw_buffer_load_b64(rq, voff + i * 1280, 0, 0);
    __builtin_amdgcn_sched_barrier(0);
    f32x2 qf[2][8];
#pragma unroll
    for (int jj = 0; jj < 2; ++jj) {
      const f32x2 dd = unpk(dr[jj]);
#pragma unroll
      for (int r = 0; r < 8; ++r) { qf[jj][r] = unpk(qq[r][jj]); e[jj][r] = e[jj][r] * dd * qf[jj][r]; }
    }
#pragma unroll
    for (int jj = 0; jj < 2; ++jj)
#pragma unroll
      for (int r = 0; r < 8; ++r) asm volatile("" : "+v"(e[jj][r]), "+v"(qf[jj][r]));
    stamp(3);
    while (lds_ld(flags + 8 + 4 * dl) < 8u) __builtin_amdgcn_s_sleep(1);
    asm volatile("" ::: "memory");
    stamp(4);
    LAS char* yr = smem + YOFF + dl * YSTR + hq * (4096 + 16) + j * 32;
    f32x4 y[4][2];
#pragma unroll
    for (int cc = 0; cc < 4; ++cc) { y[cc][0] = *(LAS f32x4*)(yr + cc * 1024); y[cc][1] = *(LAS f32x4*)(yr + cc * 1024 + 16); }
    const auto ro = __builtin_amdgcn_make_buffer_rsrc((void*)out, 0, 2 * TBYTES, 0x00020000);
    const unsigned ooff = row0 * 2560 + (half ? 1280 : 0) + ol * 16 + hq * 8;
#pragma unroll
    for (int r = 0; r < 8; ++r) {
      const float o0 = fmaf(qf[0][r].x, y[0][r >> 2][r & 3], e[0][r].x);
      const float o1 = fmaf(qf[0][r].y, y[1][r >> 2][r & 3], e[0][r].y);
      const float o2 = fmaf(qf[1][r].x, y[2][r >> 2][r & 3], e[1][r].x);
      const float o3 = fmaf(qf[1][r].y, y[3][r >> 2][r & 3], e[1][r].y);
      __builtin_amdgcn_raw_buffer_store_b64(u32x2{pk(o0, o1), pk(o2, o3)}, ro, ooff + r * 2560, 0, 0);
    }
    stamp(5);
    asm volatile("s_waitcnt vmcnt(0)" ::: "memory");
    stamp(6);
  } else {
    const int c = wv;
    const u32x4* hp = (const u32x4*)((half ? hB : hA) + (size_t)(ol * 8 + c) * 2048) + lane;
    u32x4 hv[4];
#pragma unroll
    for (int i = 0; i < 4; ++i) hv[i] = hp[i * 64];
    __builtin_amdgcn_sched_barrier(0);
    LAS char* hb = smem + c * HSTR;
    LAS char* xb = smem + XOFF + c * XSTR + (c >> 2) * 32;
    if (lane < 28) *(LAS u32x4*)(xb + lane * 16) = u32x4{0, 0, 0, 0};
    else if (lane < 32) *(LAS u32x4*)(hb + (lane - 28) * 16) = u32x4{0, 0, 0, 0};
    else if (lane < 36 && c == 0) *(LAS u32x4*)(flags + (lane - 32) * 16) = u32x4{0, 0, 0, 0};
    asm volatile("s_waitcnt lgkmcnt(0)" ::: "memory");
    __builtin_amdgcn_s_barrier();
#pragma unroll
    for (int i = 0; i < 4; ++i) *(LAS u32x4*)(hb + 64 + i * 1024 + lane * 16) = hv[i];
    stamp(1);
    LAS char* hl = hb + 64 - 32 + 2 * (16 * (lane >> 4) + 4 * ((lane & 3) - ((lane >> 2) & 3)));
    LAS char* xl = xb + 448 - 384 - 64 * (lane >> 5) + (lane & 31) * 16;
    LAS char* yw = smem + YOFF + c * 1024 + (c >> 2) * 16 + lane * 4;
    const bool l63 = lane == 63;
    float c0 = 0.f, c1 = 0.f, c2 = 0.f;
    unsigned xm = 0;
    auto waitx = [&](int d) {
      if (!((xm >> d) & 1)) {
        while (!(((xm = lds_ld(flags)) >> d) & 1)) __builtin_amdgcn_s_sleep(1);
        asm volatile("" ::: "memory");
      }
    };
    auto finish = [&](const f32x16& a, int t) {
#pragma unroll
      for (int i = 0; i < 4; ++i) {
        const float t0 = a[4 * i + 3];
        const float t1 = a[4 * i + 2] + ror1(l63 ? c0 : t0);
        const float t2 = a[4 * i + 1] + ror1(l63 ? c1 : t1);
        const float yv = a[4 * i + 0] + ror1(l63 ? c2 : t2);
        c0 = t0; c1 = t1; c2 = t2;
        *(LAS float*)(yw + t * YSTR + i * 256) = yv;
      }
      asm volatile("s_waitcnt lgkmcnt(0)" ::: "memory");
      if (lane == 0) __hip_atomic_fetch_add((LAS unsigned*)(flags + 8 + 4 * t), 1u, __ATOMIC_RELAXED, __HIP_MEMORY_SCOPE_WORKGROUP);
    };
#pragma unroll 1
    for (int t0 = 0; t0 < 8; t0 += 2) {
      f32x16 a0 = {}, a1 = {};
      bf16x8 HA[4], HB[4];
#pragma unroll
      for (int r = 0; r < 4; ++r) HA[r] = ld_h(hl + (t0 + 1) * 512 + r * 128);
      auto step = [&](int d, const bf16x8(&P)[4], bf16x8(&N)[4]) {
        waitx(d);
        LAS char* xp = xl + d * 512;
        LAS char* hq_ = hl + (t0 - d) * 512;
        bf16x8 X[4];
#pragma unroll
        for (int r = 0; r < 4; ++r) { X[r] = *(LAS bf16x8*)(xp + (3 - r) * 128); N[r] = ld_h(hq_ + r * 128); }
#pragma unroll
        for (int r = 0; r < 4; ++r) {
          a1 = __builtin_amdgcn_mfma_f32_32x32x16_bf16(X[r], P[r], a1, 0, 0, 0);
          a0 = __builtin_amdgcn_mfma_f32_32x32x16_bf16(X[r], N[r], a0, 0, 0, 0);
        }
      };
#pragma unroll 1
      for (int d = 0;; d += 2) {
        step(d, HA, HB);
        if (d == t0) break;
        step(d + 1, HB, HA);
      }
      if (t0 == 0) stamp(2);
      waitx(t0 + 1);
      {
        LAS char* xp = xl + (t0 + 1) * 512;
        bf16x8 X[4];
#pragma unroll
        for (int r = 0; r < 4; ++r) X[r] = *(LAS bf16x8*)(xp + (3 - r) * 128);
#pragma unroll
        for (int r = 0; r < 4; ++r) a1 = __builtin_amdgcn_mfma_f32_32x32x16_bf16(X[r], HB[r], a1, 0, 0, 0);
      }
      finish(a0, t0);
      finish(a1, t0 + 1);
      if (t0 == 0) stamp(3); else if (t0 == 2) stamp(4); else if (t0 == 4) stamp(5); else stamp(6);
    }
  }
  if (b == 0 && wv == DIAGW) {
    const u64 target = T[0] + 4000 + 4 * (T[DIAGS] - T[0]);
    while (__builtin_amdgcn_s_memrealtime() < target) __builtin_amdgcn_s_sleep(1);
  }
}

extern "C" void kernel_launch(void* const* d_in, const int* in_sizes, int n_in, void* d_out, int out_size,
                              void* d_ws, size_t ws_size, hipStream_t stream) {
  const u16* const* in = (const u16* const*)d_in;
  hipFuncSetAttribute((const void*)h3_kernel, hipFuncAttributeMaxDynamicSharedMemorySize, LDS_BYTES);
  h3_kernel<<<8 * (20 + NHELP), 1024, LDS_BYTES, stream>>>(in[0], in[1], in[2], in[3], in[4], in[5], in[6], in[7], in[8],
                                                          in[9], in[10], in[11], (u16*)d_out);
}
```

```cpp
#include <hip/hip_runtime.h>
#include <stdint.h>

#define MFMA_PROGRAM \
  "v_and_b32 v6, 3, %[ln]\n" \
  "v_bfe_u32 v7, %[ln], 2, 2\n" \
  "v_lshrrev_b32 v8, 4, %[ln]\n" \
  "v_sub_u32 v9, v6, v7\n" \
  "v_lshl_add_u32 v9, v8, 2, v9\n" \
  "v_lshl_add_u32 v1, v9, 3, %[hb]\n" \
  "v_lshl_add_u32 v9, v8, 2, v6\n" \
  "v_lshl_add_u32 v9, v9, 2, v7\n" \
  "v_lshl_add_u32 v2, v9, 2, %[yb]\n" \
  "v_bfe_u32 v7, %[ln], 2, 3\n" \
  "v_lshrrev_b32 v8, 5, %[ln]\n" \
  "v_sub_u32 v7, v7, v8\n" \
  "v_lshl_add_u32 v8, v7, 6, %[xb]\n" \
  "v_ashrrev_i32 v7, 1, v7\n" \
  "v_subrev_u32 v9, 0, v7\n" \
  "v_xor_b32 v9, v9, v6\n" \
  "v_and_b32 v9, 3, v9\n" \
  "v_lshl_add_u32 v4, v9, 4, v8\n" \
  "v_subrev_u32 v9, 1, v7\n" \
  "v_xor_b32 v9, v9, v6\n" \
  "v_and_b32 v9, 3, v9\n" \
  "v_lshl_add_u32 v5, v9, 4, v8\n" \
  "v_subrev_u32 v9, 2, v7\n" \
  "v_xor_b32 v9, v9, v6\n" \
  "v_and_b32 v9, 3, v9\n" \
  "v_lshl_add_u32 v124, v9, 4, v8\n" \
  "v_subrev_u32 v9, 3, v7\n" \
  "v_xor_b32 v9, v9, v6\n" \
  "v_and_b32 v9, 3, v9\n" \
  "v_lshl_add_u32 v125, v9, 4, v8\n" \
  "v_min_u32 v9, 1, %[ln]\n" \
  "v_lshl_add_u32 v9, v9, 5, %[ln]\n" \
  "v_lshl_add_u32 v3, v9, 3, %[fb]\n" \
  "s_waitcnt lgkmcnt(0)\n" \
  "v_mov_b32 v118, 0\n" \
  "v_mov_b32 v120, 0\n" \
  "v_mov_b32 v122, 0\n" \
  "ds_read_u8 v123, v3 offset:128\n" \
  "s_waitcnt lgkmcnt(0)\n" \
  "v_readfirstlane_b32 s20, v123\n" \
  "s_bitcmp1_b32 s20, 0\n" \
  "s_cbranch_scc0 .Lslow8_%=\n" \
  ".Lback8_%=:\n" \
  "ds_read_b64_tr_b16 v[54:55], v1 offset:32\n" \
  "ds_read_b64_tr_b16 v[56:57], v1 offset:0\n" \
  "ds_read_b64_tr_b16 v[58:59], v1 offset:160\n" \
  "ds_read_b64_tr_b16 v[60:61], v1 offset:128\n" \
  "ds_read_b64_tr_b16 v[62:63], v1 offset:288\n" \
  "ds_read_b64_tr_b16 v[64:65], v1 offset:256\n" \
  "ds_read_b64_tr_b16 v[66:67], v1 offset:416\n" \
  "ds_read_b64_tr_b16 v[68:69], v1 offset:384\n" \
  "ds_read_b64_tr_b16 v[70:71], v1 offset:544\n" \
  "ds_read_b64_tr_b16 v[72:73], v1 offset:512\n" \
  "ds_read_b64_tr_b16 v[74:75], v1 offset:1568\n" \
  "ds_read_b64_tr_b16 v[76:77], v1 offset:1536\n" \
  "ds_read_b64_tr_b16 v[78:79], v1 offset:672\n" \
  "ds_read_b64_tr_b16 v[80:81], v1 offset:640\n" \
  "ds_read_b64_tr_b16 v[82:83], v1 offset:1696\n" \
  "ds_read_b64_tr_b16 v[84:85], v1 offset:1664\n" \
  "ds_read_u8 v123, v3 offset:64\n" \
  "s_waitcnt lgkmcnt(0)\n" \
  "v_readfirstlane_b32 s20, v123\n" \
  "s_bitcmp1_b32 s20, 0\n" \
  "s_cbranch_scc0 .Lslow0_%=\n" \
  ".Lback0_%=:\n" \
  "ds_read_b128 v[86:89], v4 offset:384\n" \
  "ds_read_b128 v[90:93], v5 offset:256\n" \
  "ds_read_b128 v[94:97], v124 offset:128\n" \
  "ds_read_b128 v[98:101], v125 offset:0\n" \
  "s_waitcnt lgkmcnt(3)\n" \
  "s_setprio 3\n" \
  "v_mfma_f32_32x32x16_bf16 v[6:21], v[86:89], v[54:57], 0\n" \
  "s_waitcnt lgkmcnt(2)\n" \
  "v_mfma_f32_32x32x16_bf16 v[6:21], v[90:93], v[58:61], v[6:21]\n" \
  "s_waitcnt lgkmcnt(1)\n" \
  "v_mfma_f32_32x32x16_bf16 v[6:21], v[94:97], v[62:65], v[6:21]\n" \
  "ds_read_b64_tr_b16 v[102:103], v1 offset:800\n" \
  "ds_read_b64_tr_b16 v[104:105], v1 offset:768\n" \
  "s_waitcnt lgkmcnt(2)\n" \
  "v_mfma_f32_32x32x16_bf16 v[6:21], v[98:101], v[66:69], v[6:21]\n" \
  "ds_read_b64_tr_b16 v[106:107], v1 offset:1824\n" \
  "ds_read_b64_tr_b16 v[108:109], v1 offset:1792\n" \
  "v_mfma_f32_32x32x16_bf16 v[22:37], v[86:89], v[70:73], 0\n" \
  "ds_read_b64_tr_b16 v[110:111], v1 offset:928\n" \
  "ds_read_b64_tr_b16 v[112:113], v1 offset:896\n" \
  "s_nop 6\n" \
  "v_mfma_f32_32x32x16_bf16 v[38:53], v[86:89], v[74:77], 0\n" \
  "ds_read_b64_tr_b16 v[74:75], v1 offset:1952\n" \
  "ds_read_b64_tr_b16 v[76:77], v1 offset:1920\n" \
  "v_add_f32_dpp v114, v118, v8 wave_ror:1 row_mask:0xf bank_mask:0xf\n" \
  "v_add_f32_dpp v114, v9, v8 wave_shr:1 row_mask:0xf bank_mask:0xf\n" \
  "v_add_f32_dpp v115, v9, v12 wave_ror:1 row_mask:0xf bank_mask:0xf\n" \
  "v_add_f32_dpp v115, v13, v12 wave_shr:1 row_mask:0xf bank_mask:0xf\n" \
  "v_mfma_f32_32x32x16_bf16 v[22:37], v[90:93], v[78:81], v[22:37]\n" \
  "ds_read_b64_tr_b16 v[78:79], v1 offset:1056\n" \
  "ds_read_b64_tr_b16 v[80:81], v1 offset:1024\n" \
  "v_add_f32_dpp v116, v13, v16 wave_ror:1 row_mask:0xf bank_mask:0xf\n" \
  "v_add_f32_dpp v116, v17, v16 wave_shr:1 row_mask:0xf bank_mask:0xf\n" \
  "v_add_f32_dpp v119, v17, v20 wave_ror:1 row_mask:0xf bank_mask:0xf\n" \
  "v_add_f32_dpp v119, v21, v20 wave_shr:1 row_mask:0xf bank_mask:0xf\n" \
  "v_mfma_f32_32x32x16_bf16 v[38:53], v[90:93], v[82:85], v[38:53]\n" \
  "ds_read_b64_tr_b16 v[82:83], v1 offset:1184\n" \
  "ds_read_b64_tr_b16 v[84:85], v1 offset:1152\n" \
  "v_mov_b32 v118, v21\n" \
  "v_add_f32_dpp v8, v120, v7 wave_ror:1 row_mask:0xf bank_mask:0xf\n" \
  "v_add_f32_dpp v8, v114, v7 wave_shr:1 row_mask:0xf bank_mask:0xf\n" \
  "v_add_f32_dpp v12, v114, v11 wave_ror:1 row_mask:0xf bank_mask:0xf\n" \
  "s_waitcnt lgkmcnt(10)\n" \
  "v_mfma_f32_32x32x16_bf16 v[22:37], v[94:97], v[102:105], v[22:37]\n" \
  "ds_read_b64_tr_b16 v[102:103], v1 offset:1312\n" \
  "ds_read_b64_tr_b16 v[104:105], v1 offset:1280\n" \
  "v_add_f32_dpp v12, v115, v11 wave_shr:1 row_mask:0xf bank_mask:0xf\n" \
  "v_add_f32_dpp v16, v115, v15 wave_ror:1 row_mask:0xf bank_mask:0xf\n" \
  "v_add_f32_dpp v16, v116, v15 wave_shr:1 row_mask:0xf bank_mask:0xf\n" \
  "v_add_f32_dpp v121, v116, v19 wave_ror:1 row_mask:0xf bank_mask:0xf\n" \
  "s_waitcnt lgkmcnt(10)\n" \
  "v_mfma_f32_32x32x16_bf16 v[38:53], v[94:97], v[106:109], v[38:53]\n" \
  "ds_read_b64_tr_b16 v[106:107], v1 offset:1440\n" \
  "ds_read_b64_tr_b16 v[108:109], v1 offset:1408\n" \
  "v_add_f32_dpp v121, v119, v19 wave_shr:1 row_mask:0xf bank_mask:0xf\n" \
  "v_add_f32_dpp v114, v122, v6 wave_ror:1 row_mask:0xf bank_mask:0xf\n" \
  "v_add_f32_dpp v114, v8, v6 wave_shr:1 row_mask:0xf bank_mask:0xf\n" \
  "v_add_f32_dpp v115, v8, v10 wave_ror:1 row_mask:0xf bank_mask:0xf\n" \
  "s_waitcnt lgkmcnt(10)\n" \
  "v_mfma_f32_32x32x16_bf16 v[22:37], v[98:101], v[110:113], v[22:37]\n" \
  "ds_read_u8 v123, v3 offset:72\n" \
  "v_add_f32_dpp v115, v12, v10 wave_shr:1 row_mask:0xf bank_mask:0xf\n" \
  "ds_write2st64_b32 v2, v114, v115 offset0:0 offset1:1\n" \
  "v_add_f32_dpp v116, v12, v14 wave_ror:1 row_mask:0xf bank_mask:0xf\n" \
  "v_add_f32_dpp v116, v16, v14 wave_shr:1 row_mask:0xf bank_mask:0xf\n" \
  "v_add_f32_dpp v117, v16, v18 wave_ror:1 row_mask:0xf bank_mask:0xf\n" \
  "v_add_f32_dpp v117, v121, v18 wave_shr:1 row_mask:0xf bank_mask:0xf\n" \
  "s_waitcnt lgkmcnt(10)\n" \
  "v_mfma_f32_32x32x16_bf16 v[38:53], v[98:101], v[74:77], v[38:53]\n" \
  "ds_write2st64_b32 v2, v116, v117 offset0:2 offset1:3\n" \
  "ds_write_b8 v3, v3 offset:0\n" \
  "s_waitcnt lgkmcnt(10)\n" \
  "v_mfma_f32_32x32x16_bf16 v[6:21], v[86:89], v[78:81], 0\n" \
  "s_waitcnt lgkmcnt(8)\n" \
  "v_mfma_f32_32x32x16_bf16 v[6:21], v[90:93], v[82:85], v[6:21]\n" \
  "s_waitcnt lgkmcnt(3)\n" \
  "v_readfirstlane_b32 s20, v123\n" \
  "s_bitcmp1_b32 s20, 0\n" \
  "s_cbranch_scc0 .Lslow1_%=\n" \
  ".Lback1_%=:\n" \
  "ds_read_b128 v[110:113], v4 offset:896\n" \
  "ds_read_b128 v[74:77], v5 offset:768\n" \
  "ds_read_b128 v[86:89], v124 offset:640\n" \
  "v_mfma_f32_32x32x16_bf16 v[6:21], v[94:97], v[102:105], v[6:21]\n" \
  "ds_read_b128 v[90:93], v125 offset:512\n" \
  "v_mfma_f32_32x32x16_bf16 v[6:21], v[98:101], v[106:109], v[6:21]\n" \
  "s_waitcnt lgkmcnt(3)\n" \
  "v_mfma_f32_32x32x16_bf16 v[22:37], v[110:113], v[54:57], v[22:37]\n" \
  "ds_read_b64_tr_b16 v[94:95], v1 offset:672\n" \
  "ds_read_b64_tr_b16 v[96:97], v1 offset:640\n" \
  "s_waitcnt lgkmcnt(4)\n" \
  "v_mfma_f32_32x32x16_bf16 v[22:37], v[74:77], v[58:61], v[22:37]\n" \
  "s_waitcnt lgkmcnt(3)\n" \
  "v_mfma_f32_32x32x16_bf16 v[22:37], v[86:89], v[62:65], v[22:37]\n" \
  "ds_read_b64_tr_b16 v[98:99], v1 offset:800\n" \
  "ds_read_b64_tr_b16 v[100:101], v1 offset:768\n" \
  "s_waitcnt lgkmcnt(4)\n" \
  "v_mfma_f32_32x32x16_bf16 v[22:37], v[90:93], v[66:69], v[22:37]\n" \
  "v_mfma_f32_32x32x16_bf16 v[6:21], v[110:113], v[70:73], v[6:21]\n" \
  "ds_read_b64_tr_b16 v[70:71], v1 offset:928\n" \
  "ds_read_b64_tr_b16 v[72:73], v1 offset:896\n" \
  "s_nop 8\n" \
  "v_mfma_f32_32x32x16_bf16 v[38:53], v[110:113], v[78:81], v[38:53]\n" \
  "v_add_f32_dpp v114, v118, v24 wave_ror:1 row_mask:0xf bank_mask:0xf\n" \
  "v_add_f32_dpp v114, v25, v24 wave_shr:1 row_mask:0xf bank_mask:0xf\n" \
  "v_add_f32_dpp v115, v25, v28 wave_ror:1 row_mask:0xf bank_mask:0xf\n" \
  "v_add_f32_dpp v115, v29, v28 wave_shr:1 row_mask:0xf bank_mask:0xf\n" \
  "v_add_f32_dpp v116, v29, v32 wave_ror:1 row_mask:0xf bank_mask:0xf\n" \
  "v_add_f32_dpp v116, v33, v32 wave_shr:1 row_mask:0xf bank_mask:0xf\n" \
  "s_waitcnt lgkmcnt(4)\n" \
  "v_mfma_f32_32x32x16_bf16 v[6:21], v[74:77], v[94:97], v[6:21]\n" \
  "ds_read_u8 v123, v3 offset:80\n" \
  "v_add_f32_dpp v120, v33, v36 wave_ror:1 row_mask:0xf bank_mask:0xf\n" \
  "v_add_f32_dpp v120, v37, v36 wave_shr:1 row_mask:0xf bank_mask:0xf\n" \
  "v_mov_b32 v118, v37\n" \
  "v_add_f32_dpp v24, v119, v23 wave_ror:1 row_mask:0xf bank_mask:0xf\n" \
  "v_add_f32_dpp v24, v114, v23 wave_shr:1 row_mask:0xf bank_mask:0xf\n" \
  "v_add_f32_dpp v28, v114, v27 wave_ror:1 row_mask:0xf bank_mask:0xf\n" \
  "v_mfma_f32_32x32x16_bf16 v[38:53], v[74:77], v[82:85], v[38:53]\n" \
  "v_add_f32_dpp v28, v115, v27 wave_shr:1 row_mask:0xf bank_mask:0xf\n" \
  "v_add_f32_dpp v32, v115, v31 wave_ror:1 row_mask:0xf bank_mask:0xf\n" \
  "v_add_f32_dpp v32, v116, v31 wave_shr:1 row_mask:0xf bank_mask:0xf\n" \
  "v_add_f32_dpp v122, v116, v35 wave_ror:1 row_mask:0xf bank_mask:0xf\n" \
  "v_add_f32_dpp v122, v120, v35 wave_shr:1 row_mask:0xf bank_mask:0xf\n" \
  "v_add_f32_dpp v114, v121, v22 wave_ror:1 row_mask:0xf bank_mask:0xf\n" \
  "s_waitcnt lgkmcnt(3)\n" \
  "v_mfma_f32_32x32x16_bf16 v[6:21], v[86:89], v[98:101], v[6:21]\n" \
  "v_add_f32_dpp v114, v24, v22 wave_shr:1 row_mask:0xf bank_mask:0xf\n" \
  "v_add_f32_dpp v115, v24, v26 wave_ror:1 row_mask:0xf bank_mask:0xf\n" \
  "v_add_f32_dpp v115, v28, v26 wave_shr:1 row_mask:0xf bank_mask:0xf\n" \
  "ds_write2st64_b32 v2, v114, v115 offset0:33 offset1:34\n" \
  "v_add_f32_dpp v116, v28, v30 wave_ror:1 row_mask:0xf bank_mask:0xf\n" \
  "v_add_f32_dpp v116, v32, v30 wave_shr:1 row_mask:0xf bank_mask:0xf\n" \
  "v_mfma_f32_32x32x16_bf16 v[38:53], v[86:89], v[102:105], v[38:53]\n" \
  "s_waitcnt lgkmcnt(1)\n" \
  "v_readfirstlane_b32 s20, v123\n" \
  "s_bitcmp1_b32 s20, 0\n" \
  "s_cbranch_scc0 .Lslow2_%=\n" \
  ".Lback2_%=:\n" \
  "ds_read_b128 v[110:113], v4 offset:1408\n" \
  "ds_read_b128 v[74:77], v5 offset:1280\n" \
  "ds_read_b128 v[98:101], v124 offset:1152\n" \
  "v_add_f32_dpp v117, v32, v34 wave_ror:1 row_mask:0xf bank_mask:0xf\n" \
  "v_add_f32_dpp v117, v122, v34 wave_shr:1 row_mask:0xf bank_mask:0xf\n" \
  "ds_write2st64_b32 v2, v116, v117 offset0:35 offset1:36\n" \
  "ds_write_b8 v3, v3 offset:8\n" \
  "v_mfma_f32_32x32x16_bf16 v[6:21], v[90:93], v[70:73], v[6:21]\n" \
  "ds_read_b128 v[86:89], v125 offset:1024\n" \
  "ds_read_b64_tr_b16 v[70:71], v1 offset:544\n" \
  "ds_read_b64_tr_b16 v[72:73], v1 offset:512\n" \
  "v_mfma_f32_32x32x16_bf16 v[38:53], v[90:93], v[106:109], v[38:53]\n" \
  "s_waitcnt lgkmcnt(7)\n" \
  "v_mfma_f32_32x32x16_bf16 v[6:21], v[110:113], v[54:57], v[6:21]\n" \
  "s_waitcnt lgkmcnt(6)\n" \
  "v_mfma_f32_32x32x16_bf16 v[6:21], v[74:77], v[58:61], v[6:21]\n" \
  "s_waitcnt lgkmcnt(5)\n" \
  "v_mfma_f32_32x32x16_bf16 v[6:21], v[98:101], v[62:65], v[6:21]\n" \
  "ds_read_b64_tr_b16 v[90:91], v1 offset:800\n" \
  "ds_read_b64_tr_b16 v[92:93], v1 offset:768\n" \
  "s_waitcnt lgkmcnt(4)\n" \
  "v_mfma_f32_32x32x16_bf16 v[6:21], v[86:89], v[66:69], v[6:21]\n" \
  "s_waitcnt lgkmcnt(2)\n" \
  "v_mfma_f32_32x32x16_bf16 v[38:53], v[110:113], v[70:73], v[38:53]\n" \
  "ds_read_b64_tr_b16 v[70:71], v1 offset:928\n" \
  "ds_read_b64_tr_b16 v[72:73], v1 offset:896\n" \
  "s_nop 7\n" \
  "v_mfma_f32_32x32x16_bf16 v[22:37], v[110:113], v[78:81], 0\n" \
  "v_add_f32_dpp v114, v118, v8 wave_ror:1 row_mask:0xf bank_mask:0xf\n" \
  "v_add_f32_dpp v114, v9, v8 wave_shr:1 row_mask:0xf bank_mask:0xf\n" \
  "v_add_f32_dpp v115, v9, v12 wave_ror:1 row_mask:0xf bank_mask:0xf\n" \
  "v_add_f32_dpp v115, v13, v12 wave_shr:1 row_mask:0xf bank_mask:0xf\n" \
  "v_add_f32_dpp v116, v13, v16 wave_ror:1 row_mask:0xf bank_mask:0xf\n" \
  "v_add_f32_dpp v116, v17, v16 wave_shr:1 row_mask:0xf bank_mask:0xf\n" \
  "v_mfma_f32_32x32x16_bf16 v[38:53], v[74:77], v[94:97], v[38:53]\n" \
  "ds_read_b64_tr_b16 v[94:95], v1 offset:1568\n" \
  "ds_read_b64_tr_b16 v[96:97], v1 offset:1536\n" \
  "v_add_f32_dpp v119, v17, v20 wave_ror:1 row_mask:0xf bank_mask:0xf\n" \
  "v_add_f32_dpp v119, v21, v20 wave_shr:1 row_mask:0xf bank_mask:0xf\n" \
  "v_mov_b32 v118, v21\n" \
  "v_add_f32_dpp v8, v120, v7 wave_ror:1 row_mask:0xf bank_mask:0xf\n" \
  "v_mfma_f32_32x32x16_bf16 v[22:37], v[74:77], v[82:85], v[22:37]\n" \
  "ds_read_b64_tr_b16 v[82:83], v1 offset:1696\n" \
  "ds_read_b64_tr_b16 v[84:85], v1 offset:1664\n" \
  "v_add_f32_dpp v8, v114, v7 wave_shr:1 row_mask:0xf bank_mask:0xf\n" \
  "v_add_f32_dpp v12, v114, v11 wave_ror:1 row_mask:0xf bank_mask:0xf\n" \
  "v_add_f32_dpp v12, v115, v11 wave_shr:1 row_mask:0xf bank_mask:0xf\n" \
  "v_add_f32_dpp v16, v115, v15 wave_ror:1 row_mask:0xf bank_mask:0xf\n" \
  "s_waitcnt lgkmcnt(6)\n" \
  "v_mfma_f32_32x32x16_bf16 v[38:53], v[98:101], v[90:93], v[38:53]\n" \
  "ds_read_b64_tr_b16 v[90:91], v1 offset:1824\n" \
  "ds_read_b64_tr_b16 v[92:93], v1 offset:1792\n" \
  "v_add_f32_dpp v16, v116, v15 wave_shr:1 row_mask:0xf bank_mask:0xf\n" \
  "v_add_f32_dpp v121, v116, v19 wave_ror:1 row_mask:0xf bank_mask:0xf\n" \
  "v_add_f32_dpp v121, v119, v19 wave_shr:1 row_mask:0xf bank_mask:0xf\n" \
  "v_add_f32_dpp v114, v122, v6 wave_ror:1 row_mask:0xf bank_mask:0xf\n" \
  "v_mfma_f32_32x32x16_bf16 v[22:37], v[98:101], v[102:105], v[22:37]\n" \
  "ds_read_b64_tr_b16 v[102:103], v1 offset:1952\n" \
  "ds_read_b64_tr_b16 v[104:105], v1 offset:1920\n" \
  "v_add_f32_dpp v114, v8, v6 wave_shr:1 row_mask:0xf bank_mask:0xf\n" \
  "v_add_f32_dpp v115, v8, v10 wave_ror:1 row_mask:0xf bank_mask:0xf\n" \
  "v_add_f32_dpp v115, v12, v10 wave_shr:1 row_mask:0xf bank_mask:0xf\n" \
  "ds_write2st64_b32 v2, v114, v115 offset0:66 offset1:67\n" \
  "s_waitcnt lgkmcnt(9)\n" \
  "v_mfma_f32_32x32x16_bf16 v[38:53], v[86:89], v[70:73], v[38:53]\n" \
  "ds_read_u8 v123, v3 offset:88\n" \
  "v_add_f32_dpp v116, v12, v14 wave_ror:1 row_mask:0xf bank_mask:0xf\n" \
  "v_add_f32_dpp v116, v16, v14 wave_shr:1 row_mask:0xf bank_mask:0xf\n" \
  "v_add_f32_dpp v117, v16, v18 wave_ror:1 row_mask:0xf bank_mask:0xf\n" \
  "v_add_f32_dpp v117, v121, v18 wave_shr:1 row_mask:0xf bank_mask:0xf\n" \
  "ds_write2st64_b32 v2, v116, v117 offset0:68 offset1:69\n" \
  "ds_write_b8 v3, v3 offset:16\n" \
  "v_mfma_f32_32x32x16_bf16 v[22:37], v[86:89], v[106:109], v[22:37]\n" \
  "s_waitcnt lgkmcnt(10)\n" \
  "v_mfma_f32_32x32x16_bf16 v[6:21], v[110:113], v[94:97], 0\n" \
  "s_waitcnt lgkmcnt(8)\n" \
  "v_mfma_f32_32x32x16_bf16 v[6:21], v[74:77], v[82:85], v[6:21]\n" \
  "s_waitcnt lgkmcnt(2)\n" \
  "v_readfirstlane_b32 s20, v123\n" \
  "s_bitcmp1_b32 s20, 0\n" \
  "s_cbranch_scc0 .Lslow3_%=\n" \
  ".Lback3_%=:\n" \
  "ds_read_b128 v[70:73], v4 offset:1920\n" \
  "ds_read_b128 v[106:109], v5 offset:1792\n" \
  "ds_read_b128 v[110:113], v124 offset:1664\n" \
  "v_mfma_f32_32x32x16_bf16 v[6:21], v[98:101], v[90:93], v[6:21]\n" \
  "ds_read_b128 v[74:77], v125 offset:1536\n" \
  "ds_read_b64_tr_b16 v[98:99], v1 offset:544\n" \
  "ds_read_b64_tr_b16 v[100:101], v1 offset:512\n" \
  "v_mfma_f32_32x32x16_bf16 v[6:21], v[86:89], v[102:105], v[6:21]\n" \
  "s_waitcnt lgkmcnt(5)\n" \
  "v_mfma_f32_32x32x16_bf16 v[38:53], v[70:73], v[54:57], v[38:53]\n" \
  "ds_read_b64_tr_b16 v[86:87], v1 offset:672\n" \
  "ds_read_b64_tr_b16 v[88:89], v1 offset:640\n" \
  "s_waitcnt lgkmcnt(6)\n" \
  "v_mfma_f32_32x32x16_bf16 v[38:53], v[106:109], v[58:61], v[38:53]\n" \
  "ds_read_b64_tr_b16 v[58:59], v1 offset:1184\n" \
  "ds_read_b64_tr_b16 v[60:61], v1 offset:1152\n" \
  "s_waitcnt lgkmcnt(7)\n" \
  "v_mfma_f32_32x32x16_bf16 v[38:53], v[110:113], v[62:65], v[38:53]\n" \
  "ds_read_b64_tr_b16 v[62:63], v1 offset:800\n" \
  "ds_read_b64_tr_b16 v[64:65], v1 offset:768\n" \
  "s_waitcnt lgkmcnt(8)\n" \
  "v_mfma_f32_32x32x16_bf16 v[38:53], v[74:77], v[66:69], v[38:53]\n" \
  "ds_read_b64_tr_b16 v[66:67], v1 offset:1312\n" \
  "ds_read_b64_tr_b16 v[68:69], v1 offset:1280\n" \
  "s_waitcnt lgkmcnt(8)\n" \
  "v_mfma_f32_32x32x16_bf16 v[22:37], v[70:73], v[98:101], v[22:37]\n" \
  "ds_read_u8 v123, v3 offset:136\n" \
  "ds_read_b64_tr_b16 v[98:99], v1 offset:928\n" \
  "ds_read_b64_tr_b16 v[100:101], v1 offset:896\n" \
  "s_nop 4\n" \
  "v_mfma_f32_32x32x16_bf16 v[6:21], v[70:73], v[78:81], v[6:21]\n" \
  "ds_read_b64_tr_b16 v[70:71], v1 offset:1440\n" \
  "ds_read_b64_tr_b16 v[72:73], v1 offset:1408\n" \
  "v_add_f32_dpp v114, v118, v40 wave_ror:1 row_mask:0xf bank_mask:0xf\n" \
  "v_add_f32_dpp v114, v41, v40 wave_shr:1 row_mask:0xf bank_mask:0xf\n" \
  "v_add_f32_dpp v115, v41, v44 wave_ror:1 row_mask:0xf bank_mask:0xf\n" \
  "v_add_f32_dpp v115, v45, v44 wave_shr:1 row_mask:0xf bank_mask:0xf\n" \
  "s_waitcnt lgkmcnt(11)\n" \
  "v_mfma_f32_32x32x16_bf16 v[22:37], v[106:109], v[86:89], v[22:37]\n" \
  "ds_read_b128 v[86:89], v4 offset:896\n" \
  "v_add_f32_dpp v116, v45, v48 wave_ror:1 row_mask:0xf bank_mask:0xf\n" \
  "v_add_f32_dpp v116, v49, v48 wave_shr:1 row_mask:0xf bank_mask:0xf\n" \
  "v_add_f32_dpp v120, v49, v52 wave_ror:1 row_mask:0xf bank_mask:0xf\n" \
  "v_add_f32_dpp v120, v53, v52 wave_shr:1 row_mask:0xf bank_mask:0xf\n" \
  "v_mov_b32 v118, v53\n" \
  "s_waitcnt lgkmcnt(10)\n" \
  "v_mfma_f32_32x32x16_bf16 v[6:21], v[106:109], v[58:61], v[6:21]\n" \
  "s_waitcnt lgkmcnt(5)\n" \
  "v_readfirstlane_b32 s20, v123\n" \
  "s_bitcmp1_b32 s20, 0\n" \
  "s_cbranch_scc0 .Lslow9_%=\n" \
  ".Lback9_%=:\n" \
  "ds_read_b64_tr_b16 v[106:107], v1 offset:2080\n" \
  "ds_read_b64_tr_b16 v[108:109], v1 offset:2048\n" \
  "v_add_f32_dpp v40, v119, v39 wave_ror:1 row_mask:0xf bank_mask:0xf\n" \
  "v_add_f32_dpp v40, v114, v39 wave_shr:1 row_mask:0xf bank_mask:0xf\n" \
  "v_add_f32_dpp v44, v114, v43 wave_ror:1 row_mask:0xf bank_mask:0xf\n" \
  "v_add_f32_dpp v44, v115, v43 wave_shr:1 row_mask:0xf bank_mask:0xf\n" \
  "v_mfma_f32_32x32x16_bf16 v[22:37], v[110:113], v[62:65], v[22:37]\n" \
  "ds_read_b128 v[62:65], v5 offset:768\n" \
  "v_add_f32_dpp v48, v115, v47 wave_ror:1 row_mask:0xf bank_mask:0xf\n" \
  "v_add_f32_dpp v48, v116, v47 wave_shr:1 row_mask:0xf bank_mask:0xf\n" \
  "v_add_f32_dpp v122, v116, v51 wave_ror:1 row_mask:0xf bank_mask:0xf\n" \
  "v_add_f32_dpp v122, v120, v51 wave_shr:1 row_mask:0xf bank_mask:0xf\n" \
  "v_add_f32_dpp v114, v121, v38 wave_ror:1 row_mask:0xf bank_mask:0xf\n" \
  "v_mfma_f32_32x32x16_bf16 v[6:21], v[110:113], v[66:69], v[6:21]\n" \
  "ds_read_b64_tr_b16 v[110:111], v1 offset:2208\n" \
  "ds_read_b64_tr_b16 v[112:113], v1 offset:2176\n" \
  "v_add_f32_dpp v114, v40, v38 wave_shr:1 row_mask:0xf bank_mask:0xf\n" \
  "v_add_f32_dpp v115, v40, v42 wave_ror:1 row_mask:0xf bank_mask:0xf\n" \
  "v_add_f32_dpp v115, v44, v42 wave_shr:1 row_mask:0xf bank_mask:0xf\n" \
  "ds_write2st64_b32 v2, v114, v115 offset0:99 offset1:100\n" \
  "s_waitcnt lgkmcnt(9)\n" \
  "v_mfma_f32_32x32x16_bf16 v[22:37], v[74:77], v[98:101], v[22:37]\n" \
  "ds_read_b128 v[98:101], v124 offset:640\n" \
  "v_add_f32_dpp v116, v44, v46 wave_ror:1 row_mask:0xf bank_mask:0xf\n" \
  "v_add_f32_dpp v116, v48, v46 wave_shr:1 row_mask:0xf bank_mask:0xf\n" \
  "v_add_f32_dpp v117, v48, v50 wave_ror:1 row_mask:0xf bank_mask:0xf\n" \
  "v_add_f32_dpp v117, v122, v50 wave_shr:1 row_mask:0xf bank_mask:0xf\n" \
  "ds_write2st64_b32 v2, v116, v117 offset0:101 offset1:102\n" \
  "s_waitcnt lgkmcnt(9)\n" \
  "v_mfma_f32_32x32x16_bf16 v[6:21], v[74:77], v[70:73], v[6:21]\n" \
  "ds_read_b64_tr_b16 v[74:75], v1 offset:2336\n" \
  "ds_read_b64_tr_b16 v[76:77], v1 offset:2304\n" \
  "ds_write_b8 v3, v3 offset:24\n" \
  "s_waitcnt lgkmcnt(11)\n" \
  "s_setprio 2\n" \
  "v_mfma_f32_32x32x16_bf16 v[22:37], v[86:89], v[94:97], v[22:37]\n" \
  "ds_read_b128 v[70:73], v125 offset:512\n" \
  "s_waitcnt lgkmcnt(10)\n" \
  "v_mfma_f32_32x32x16_bf16 v[6:21], v[86:89], v[106:109], v[6:21]\n" \
  "ds_read_b64_tr_b16 v[94:95], v1 offset:2464\n" \
  "ds_read_b64_tr_b16 v[96:97], v1 offset:2432\n" \
  "s_waitcnt lgkmcnt(11)\n" \
  "v_mfma_f32_32x32x16_bf16 v[22:37], v[62:65], v[82:85], v[22:37]\n" \
  "ds_read_b128 v[86:89], v4 offset:384\n" \
  "s_waitcnt lgkmcnt(10)\n" \
  "v_mfma_f32_32x32x16_bf16 v[6:21], v[62:65], v[110:113], v[6:21]\n" \
  "ds_read_b64_tr_b16 v[82:83], v1 offset:2592\n" \
  "ds_read_b64_tr_b16 v[84:85], v1 offset:2560\n" \
  "s_waitcnt lgkmcnt(10)\n" \
  "v_mfma_f32_32x32x16_bf16 v[22:37], v[98:101], v[90:93], v[22:37]\n" \
  "ds_read_b128 v[62:65], v5 offset:256\n" \
  "s_waitcnt lgkmcnt(8)\n" \
  "v_mfma_f32_32x32x16_bf16 v[6:21], v[98:101], v[74:77], v[6:21]\n" \
  "ds_read_b64_tr_b16 v[90:91], v1 offset:2720\n" \
  "ds_read_b64_tr_b16 v[92:93], v1 offset:2688\n" \
  "s_waitcnt lgkmcnt(8)\n" \
  "v_mfma_f32_32x32x16_bf16 v[22:37], v[70:73], v[102:105], v[22:37]\n" \
  "ds_read_b128 v[98:101], v124 offset:128\n" \
  "s_waitcnt lgkmcnt(7)\n" \
  "v_mfma_f32_32x32x16_bf16 v[6:21], v[70:73], v[94:97], v[6:21]\n" \
  "ds_read_u8 v123, v3 offset:96\n" \
  "ds_read_b64_tr_b16 v[102:103], v1 offset:2848\n" \
  "ds_read_b64_tr_b16 v[104:105], v1 offset:2816\n" \
  "s_waitcnt lgkmcnt(9)\n" \
  "v_mfma_f32_32x32x16_bf16 v[22:37], v[86:89], v[106:109], v[22:37]\n" \
  "ds_read_b128 v[70:73], v125 offset:0\n" \
  "s_waitcnt lgkmcnt(8)\n" \
  "v_mfma_f32_32x32x16_bf16 v[6:21], v[86:89], v[82:85], v[6:21]\n" \
  "ds_read_b64_tr_b16 v[106:107], v1 offset:2976\n" \
  "ds_read_b64_tr_b16 v[108:109], v1 offset:2944\n" \
  "s_waitcnt lgkmcnt(9)\n" \
  "v_mfma_f32_32x32x16_bf16 v[22:37], v[62:65], v[110:113], v[22:37]\n" \
  "s_waitcnt lgkmcnt(5)\n" \
  "v_readfirstlane_b32 s20, v123\n" \
  "s_bitcmp1_b32 s20, 0\n" \
  "s_cbranch_scc0 .Lslow4_%=\n" \
  ".Lback4_%=:\n" \
  "ds_read_b128 v[86:89], v4 offset:2432\n" \
  "v_mfma_f32_32x32x16_bf16 v[6:21], v[62:65], v[90:93], v[6:21]\n" \
  "ds_read_b128 v[82:85], v5 offset:2304\n" \
  "ds_read_b64_tr_b16 v[110:111], v1 offset:160\n" \
  "ds_read_b64_tr_b16 v[112:113], v1 offset:128\n" \
  "v_mfma_f32_32x32x16_bf16 v[22:37], v[98:101], v[74:77], v[22:37]\n" \
  "ds_read_b128 v[62:65], v124 offset:2176\n" \
  "ds_read_b64_tr_b16 v[90:91], v1 offset:288\n" \
  "ds_read_b64_tr_b16 v[92:93], v1 offset:256\n" \
  "s_waitcnt lgkmcnt(10)\n" \
  "v_mfma_f32_32x32x16_bf16 v[6:21], v[98:101], v[102:105], v[6:21]\n" \
  "ds_read_b128 v[74:77], v125 offset:2048\n" \
  "ds_read_b64_tr_b16 v[98:99], v1 offset:416\n" \
  "ds_read_b64_tr_b16 v[100:101], v1 offset:384\n" \
  "s_waitcnt lgkmcnt(12)\n" \
  "v_mfma_f32_32x32x16_bf16 v[22:37], v[70:73], v[94:97], v[22:37]\n" \
  "ds_read_b64_tr_b16 v[102:103], v1 offset:544\n" \
  "ds_read_b64_tr_b16 v[104:105], v1 offset:512\n" \
  "s_waitcnt lgkmcnt(12)\n" \
  "v_mfma_f32_32x32x16_bf16 v[6:21], v[70:73], v[106:109], v[6:21]\n" \
  "s_waitcnt lgkmcnt(11)\n" \
  "v_mfma_f32_32x32x16_bf16 v[22:37], v[86:89], v[54:57], v[22:37]\n" \
  "ds_read_b64_tr_b16 v[94:95], v1 offset:672\n" \
  "ds_read_b64_tr_b16 v[96:97], v1 offset:640\n" \
  "s_waitcnt lgkmcnt(10)\n" \
  "v_mfma_f32_32x32x16_bf16 v[22:37], v[82:85], v[110:113], v[22:37]\n" \
  "s_waitcnt lgkmcnt(7)\n" \
  "v_mfma_f32_32x32x16_bf16 v[22:37], v[62:65], v[90:93], v[22:37]\n" \
  "ds_read_b64_tr_b16 v[70:71], v1 offset:800\n" \
  "ds_read_b64_tr_b16 v[72:73], v1 offset:768\n" \
  "s_waitcnt lgkmcnt(6)\n" \
  "v_mfma_f32_32x32x16_bf16 v[22:37], v[74:77], v[98:101], v[22:37]\n" \
  "s_waitcnt lgkmcnt(4)\n" \
  "v_mfma_f32_32x32x16_bf16 v[6:21], v[86:89], v[102:105], v[6:21]\n" \
  "ds_read_b64_tr_b16 v[106:107], v1 offset:928\n" \
  "ds_read_b64_tr_b16 v[108:109], v1 offset:896\n" \
  "s_nop 7\n" \
  "v_mfma_f32_32x32x16_bf16 v[38:53], v[86:89], v[78:81], 0\n" \
  "ds_read_b64_tr_b16 v[78:79], v1 offset:1440\n" \
  "ds_read_b64_tr_b16 v[80:81], v1 offset:1408\n" \
  "v_add_f32_dpp v114, v118, v24 wave_ror:1 row_mask:0xf bank_mask:0xf\n" \
  "v_add_f32_dpp v114, v25, v24 wave_shr:1 row_mask:0xf bank_mask:0xf\n" \
  "v_add_f32_dpp v115, v25, v28 wave_ror:1 row_mask:0xf bank_mask:0xf\n" \
  "v_add_f32_dpp v115, v29, v28 wave_shr:1 row_mask:0xf bank_mask:0xf\n" \
  "s_waitcnt lgkmcnt(6)\n" \
  "v_mfma_f32_32x32x16_bf16 v[6:21], v[82:85], v[94:97], v[6:21]\n" \
  "ds_read_b64_tr_b16 v[94:95], v1 offset:1568\n" \
  "ds_read_b64_tr_b16 v[96:97], v1 offset:1536\n" \
  "v_add_f32_dpp v116, v29, v32 wave_ror:1 row_mask:0xf bank_mask:0xf\n" \
  "v_add_f32_dpp v116, v33, v32 wave_shr:1 row_mask:0xf bank_mask:0xf\n" \
  "v_add_f32_dpp v119, v33, v36 wave_ror:1 row_mask:0xf bank_mask:0xf\n" \
  "v_add_f32_dpp v119, v37, v36 wave_shr:1 row_mask:0xf bank_mask:0xf\n" \
  "v_mfma_f32_32x32x16_bf16 v[38:53], v[82:85], v[58:61], v[38:53]\n" \
  "ds_read_u8 v123, v3 offset:104\n" \
  "ds_read_b64_tr_b16 v[58:59], v1 offset:1696\n" \
  "ds_read_b64_tr_b16 v[60:61], v1 offset:1664\n" \
  "v_mov_b32 v118, v37\n" \
  "v_add_f32_dpp v24, v120, v23 wave_ror:1 row_mask:0xf bank_mask:0xf\n" \
  "v_add_f32_dpp v24, v114, v23 wave_shr:1 row_mask:0xf bank_mask:0xf\n" \
  "v_add_f32_dpp v28, v114, v27 wave_ror:1 row_mask:0xf bank_mask:0xf\n" \
  "s_waitcnt lgkmcnt(9)\n" \
  "v_mfma_f32_32x32x16_bf16 v[6:21], v[62:65], v[70:73], v[6:21]\n" \
  "ds_read_b64_tr_b16 v[70:71], v1 offset:1824\n" \
  "ds_read_b64_tr_b16 v[72:73], v1 offset:1792\n" \
  "v_add_f32_dpp v28, v115, v27 wave_shr:1 row_mask:0xf bank_mask:0xf\n" \
  "v_add_f32_dpp v32, v115, v31 wave_ror:1 row_mask:0xf bank_mask:0xf\n" \
  "v_add_f32_dpp v32, v116, v31 wave_shr:1 row_mask:0xf bank_mask:0xf\n" \
  "v_add_f32_dpp v121, v116, v35 wave_ror:1 row_mask:0xf bank_mask:0xf\n" \
  "v_mfma_f32_32x32x16_bf16 v[38:53], v[62:65], v[66:69], v[38:53]\n" \
  "ds_read_b64_tr_b16 v[66:67], v1 offset:1952\n" \
  "ds_read_b64_tr_b16 v[68:69], v1 offset:1920\n" \
  "v_add_f32_dpp v121, v119, v35 wave_shr:1 row_mask:0xf bank_mask:0xf\n" \
  "v_add_f32_dpp v114, v122, v22 wave_ror:1 row_mask:0xf bank_mask:0xf\n" \
  "v_add_f32_dpp v114, v24, v22 wave_shr:1 row_mask:0xf bank_mask:0xf\n" \
  "v_add_f32_dpp v115, v24, v26 wave_ror:1 row_mask:0xf bank_mask:0xf\n" \
  "s_waitcnt lgkmcnt(11)\n" \
  "v_mfma_f32_32x32x16_bf16 v[6:21], v[74:77], v[106:109], v[6:21]\n" \
  "s_waitcnt lgkmcnt(6)\n" \
  "v_readfirstlane_b32 s20, v123\n" \
  "s_bitcmp1_b32 s20, 0\n" \
  "s_cbranch_scc0 .Lslow5_%=\n" \
  ".Lback5_%=:\n" \
  "ds_read_b128 v[106:109], v4 offset:2944\n" \
  "v_add_f32_dpp v115, v28, v26 wave_shr:1 row_mask:0xf bank_mask:0xf\n" \
  "ds_write2st64_b32 v2, v114, v115 offset0:132 offset1:133\n" \
  "v_add_f32_dpp v116, v28, v30 wave_ror:1 row_mask:0xf bank_mask:0xf\n" \
  "v_add_f32_dpp v116, v32, v30 wave_shr:1 row_mask:0xf bank_mask:0xf\n" \
  "v_mfma_f32_32x32x16_bf16 v[38:53], v[74:77], v[78:81], v[38:53]\n" \
  "ds_read_b128 v[78:81], v5 offset:2816\n" \
  "v_add_f32_dpp v117, v32, v34 wave_ror:1 row_mask:0xf bank_mask:0xf\n" \
  "v_add_f32_dpp v117, v121, v34 wave_shr:1 row_mask:0xf bank_mask:0xf\n" \
  "ds_write2st64_b32 v2, v116, v117 offset0:134 offset1:135\n" \
  "ds_write_b8 v3, v3 offset:32\n" \
  "v_mfma_f32_32x32x16_bf16 v[22:37], v[86:89], v[94:97], 0\n" \
  "ds_read_b128 v[86:89], v124 offset:2688\n" \
  "s_waitcnt lgkmcnt(10)\n" \
  "v_mfma_f32_32x32x16_bf16 v[22:37], v[82:85], v[58:61], v[22:37]\n" \
  "ds_read_b128 v[82:85], v125 offset:2560\n" \
  "s_waitcnt lgkmcnt(9)\n" \
  "v_mfma_f32_32x32x16_bf16 v[22:37], v[62:65], v[70:73], v[22:37]\n" \
  "s_waitcnt lgkmcnt(7)\n" \
  "v_mfma_f32_32x32x16_bf16 v[22:37], v[74:77], v[66:69], v[22:37]\n" \
  "ds_read_b64_tr_b16 v[62:63], v1 offset:1056\n" \
  "ds_read_b64_tr_b16 v[64:65], v1 offset:1024\n" \
  "s_waitcnt lgkmcnt(8)\n" \
  "v_mfma_f32_32x32x16_bf16 v[6:21], v[106:109], v[54:57], v[6:21]\n" \
  "ds_read_b64_tr_b16 v[74:75], v1 offset:672\n" \
  "ds_read_b64_tr_b16 v[76:77], v1 offset:640\n" \
  "s_waitcnt lgkmcnt(8)\n" \
  "v_mfma_f32_32x32x16_bf16 v[6:21], v[78:81], v[110:113], v[6:21]\n" \
  "ds_read_b64_tr_b16 v[54:55], v1 offset:1184\n" \
  "ds_read_b64_tr_b16 v[56:57], v1 offset:1152\n" \
  "s_waitcnt lgkmcnt(7)\n" \
  "v_mfma_f32_32x32x16_bf16 v[6:21], v[86:89], v[90:93], v[6:21]\n" \
  "ds_read_b64_tr_b16 v[110:111], v1 offset:800\n" \
  "ds_read_b64_tr_b16 v[112:113], v1 offset:768\n" \
  "s_waitcnt lgkmcnt(8)\n" \
  "v_mfma_f32_32x32x16_bf16 v[6:21], v[82:85], v[98:101], v[6:21]\n" \
  "ds_read_b64_tr_b16 v[90:91], v1 offset:1312\n" \
  "ds_read_b64_tr_b16 v[92:93], v1 offset:1280\n" \
  "v_mfma_f32_32x32x16_bf16 v[38:53], v[106:109], v[102:105], v[38:53]\n" \
  "ds_read_b64_tr_b16 v[98:99], v1 offset:928\n" \
  "ds_read_b64_tr_b16 v[100:101], v1 offset:896\n" \
  "s_nop 6\n" \
  "s_waitcnt lgkmcnt(10)\n" \
  "v_mfma_f32_32x32x16_bf16 v[22:37], v[106:109], v[62:65], v[22:37]\n" \
  "ds_read_b64_tr_b16 v[102:103], v1 offset:1440\n" \
  "ds_read_b64_tr_b16 v[104:105], v1 offset:1408\n" \
  "v_add_f32_dpp v114, v118, v8 wave_ror:1 row_mask:0xf bank_mask:0xf\n" \
  "v_add_f32_dpp v114, v9, v8 wave_shr:1 row_mask:0xf bank_mask:0xf\n" \
  "v_add_f32_dpp v115, v9, v12 wave_ror:1 row_mask:0xf bank_mask:0xf\n" \
  "v_add_f32_dpp v115, v13, v12 wave_shr:1 row_mask:0xf bank_mask:0xf\n" \
  "s_waitcnt lgkmcnt(10)\n" \
  "v_mfma_f32_32x32x16_bf16 v[38:53], v[78:81], v[74:77], v[38:53]\n" \
  "ds_read_b128 v[106:109], v4 offset:1920\n" \
  "v_add_f32_dpp v116, v13, v16 wave_ror:1 row_mask:0xf bank_mask:0xf\n" \
  "v_add_f32_dpp v116, v17, v16 wave_shr:1 row_mask:0xf bank_mask:0xf\n" \
  "v_add_f32_dpp v120, v17, v20 wave_ror:1 row_mask:0xf bank_mask:0xf\n" \
  "v_add_f32_dpp v120, v21, v20 wave_shr:1 row_mask:0xf bank_mask:0xf\n" \
  "v_mov_b32 v118, v21\n" \
  "s_waitcnt lgkmcnt(9)\n" \
  "v_mfma_f32_32x32x16_bf16 v[22:37], v[78:81], v[54:57], v[22:37]\n" \
  "ds_read_b64_tr_b16 v[62:63], v1 offset:2080\n" \
  "ds_read_b64_tr_b16 v[64:65], v1 offset:2048\n" \
  "v_add_f32_dpp v8, v119, v7 wave_ror:1 row_mask:0xf bank_mask:0xf\n" \
  "v_add_f32_dpp v8, v114, v7 wave_shr:1 row_mask:0xf bank_mask:0xf\n" \
  "v_add_f32_dpp v12, v114, v11 wave_ror:1 row_mask:0xf bank_mask:0xf\n" \
  "v_add_f32_dpp v12, v115, v11 wave_shr:1 row_mask:0xf bank_mask:0xf\n" \
  "s_waitcnt lgkmcnt(9)\n" \
  "v_mfma_f32_32x32x16_bf16 v[38:53], v[86:89], v[110:113], v[38:53]\n" \
  "ds_read_b128 v[74:77], v5 offset:1792\n" \
  "v_add_f32_dpp v16, v115, v15 wave_ror:1 row_mask:0xf bank_mask:0xf\n" \
  "v_add_f32_dpp v16, v116, v15 wave_shr:1 row_mask:0xf bank_mask:0xf\n" \
  "v_add_f32_dpp v122, v116, v19 wave_ror:1 row_mask:0xf bank_mask:0xf\n" \
  "v_add_f32_dpp v122, v120, v19 wave_shr:1 row_mask:0xf bank_mask:0xf\n" \
  "v_add_f32_dpp v114, v121, v6 wave_ror:1 row_mask:0xf bank_mask:0xf\n" \
  "s_waitcnt lgkmcnt(8)\n" \
  "v_mfma_f32_32x32x16_bf16 v[22:37], v[86:89], v[90:93], v[22:37]\n" \
  "ds_read_b64_tr_b16 v[78:79], v1 offset:2208\n" \
  "ds_read_b64_tr_b16 v[80:81], v1 offset:2176\n" \
  "v_add_f32_dpp v114, v8, v6 wave_shr:1 row_mask:0xf bank_mask:0xf\n" \
  "v_add_f32_dpp v115, v8, v10 wave_ror:1 row_mask:0xf bank_mask:0xf\n" \
  "v_add_f32_dpp v115, v12, v10 wave_shr:1 row_mask:0xf bank_mask:0xf\n" \
  "ds_write2st64_b32 v2, v114, v115 offset0:165 offset1:166\n" \
  "s_waitcnt lgkmcnt(9)\n" \
  "v_mfma_f32_32x32x16_bf16 v[38:53], v[82:85], v[98:101], v[38:53]\n" \
  "ds_read_b128 v[54:57], v124 offset:1664\n" \
  "v_add_f32_dpp v116, v12, v14 wave_ror:1 row_mask:0xf bank_mask:0xf\n" \
  "v_add_f32_dpp v116, v16, v14 wave_shr:1 row_mask:0xf bank_mask:0xf\n" \
  "v_add_f32_dpp v117, v16, v18 wave_ror:1 row_mask:0xf bank_mask:0xf\n" \
  "v_add_f32_dpp v117, v122, v18 wave_shr:1 row_mask:0xf bank_mask:0xf\n" \
  "ds_write2st64_b32 v2, v116, v117 offset0:167 offset1:168\n" \
  "s_waitcnt lgkmcnt(9)\n" \
  "v_mfma_f32_32x32x16_bf16 v[22:37], v[82:85], v[102:105], v[22:37]\n" \
  "ds_read_b64_tr_b16 v[110:111], v1 offset:2336\n" \
  "ds_read_b64_tr_b16 v[112:113], v1 offset:2304\n" \
  "ds_write_b8 v3, v3 offset:40\n" \
  "s_waitcnt lgkmcnt(11)\n" \
  "s_setprio 1\n" \
  "v_mfma_f32_32x32x16_bf16 v[38:53], v[106:109], v[94:97], v[38:53]\n" \
  "ds_read_b128 v[86:89], v125 offset:1536\n" \
  "s_waitcnt lgkmcnt(10)\n" \
  "v_mfma_f32_32x32x16_bf16 v[22:37], v[106:109], v[62:65], v[22:37]\n" \
  "ds_read_b64_tr_b16 v[90:91], v1 offset:2464\n" \
  "ds_read_b64_tr_b16 v[92:93], v1 offset:2432\n" \
  "s_waitcnt lgkmcnt(11)\n" \
  "v_mfma_f32_32x32x16_bf16 v[38:53], v[74:77], v[58:61], v[38:53]\n" \
  "ds_read_b128 v[98:101], v4 offset:1408\n" \
  "s_waitcnt lgkmcnt(10)\n" \
  "v_mfma_f32_32x32x16_bf16 v[22:37], v[74:77], v[78:81], v[22:37]\n" \
  "ds_read_b64_tr_b16 v[82:83], v1 offset:2592\n" \
  "ds_read_b64_tr_b16 v[84:85], v1 offset:2560\n" \
  "s_waitcnt lgkmcnt(10)\n" \
  "v_mfma_f32_32x32x16_bf16 v[38:53], v[54:57], v[70:73], v[38:53]\n" \
  "ds_read_b128 v[102:105], v5 offset:1280\n" \
  "s_waitcnt lgkmcnt(8)\n" \
  "v_mfma_f32_32x32x16_bf16 v[22:37], v[54:57], v[110:113], v[22:37]\n" \
  "ds_read_b64_tr_b16 v[94:95], v1 offset:2720\n" \
  "ds_read_b64_tr_b16 v[96:97], v1 offset:2688\n" \
  "s_waitcnt lgkmcnt(8)\n" \
  "v_mfma_f32_32x32x16_bf16 v[38:53], v[86:89], v[66:69], v[38:53]\n" \
  "ds_read_b128 v[106:109], v124 offset:1152\n" \
  "s_waitcnt lgkmcnt(7)\n" \
  "v_mfma_f32_32x32x16_bf16 v[22:37], v[86:89], v[90:93], v[22:37]\n" \
  "ds_read_b64_tr_b16 v[58:59], v1 offset:2848\n" \
  "ds_read_b64_tr_b16 v[60:61], v1 offset:2816\n" \
  "s_waitcnt lgkmcnt(8)\n" \
  "v_mfma_f32_32x32x16_bf16 v[38:53], v[98:101], v[62:65], v[38:53]\n" \
  "ds_read_b128 v[74:77], v125 offset:1024\n" \
  "s_waitcnt lgkmcnt(7)\n" \
  "v_mfma_f32_32x32x16_bf16 v[22:37], v[98:101], v[82:85], v[22:37]\n" \
  "ds_read_b64_tr_b16 v[70:71], v1 offset:2976\n" \
  "ds_read_b64_tr_b16 v[72:73], v1 offset:2944\n" \
  "s_waitcnt lgkmcnt(8)\n" \
  "v_mfma_f32_32x32x16_bf16 v[38:53], v[102:105], v[78:81], v[38:53]\n" \
  "ds_read_b128 v[54:57], v4 offset:896\n" \
  "s_waitcnt lgkmcnt(7)\n" \
  "v_mfma_f32_32x32x16_bf16 v[22:37], v[102:105], v[94:97], v[22:37]\n" \
  "ds_read_b64_tr_b16 v[66:67], v1 offset:3104\n" \
  "ds_read_b64_tr_b16 v[68:69], v1 offset:3072\n" \
  "s_waitcnt lgkmcnt(8)\n" \
  "v_mfma_f32_32x32x16_bf16 v[38:53], v[106:109], v[110:113], v[38:53]\n" \
  "ds_read_b128 v[86:89], v5 offset:768\n" \
  "s_waitcnt lgkmcnt(7)\n" \
  "v_mfma_f32_32x32x16_bf16 v[22:37], v[106:109], v[58:61], v[22:37]\n" \
  "ds_read_b64_tr_b16 v[62:63], v1 offset:3232\n" \
  "ds_read_b64_tr_b16 v[64:65], v1 offset:3200\n" \
  "s_waitcnt lgkmcnt(8)\n" \
  "v_mfma_f32_32x32x16_bf16 v[38:53], v[74:77], v[90:93], v[38:53]\n" \
  "ds_read_b128 v[98:101], v124 offset:640\n" \
  "s_waitcnt lgkmcnt(7)\n" \
  "v_mfma_f32_32x32x16_bf16 v[22:37], v[74:77], v[70:73], v[22:37]\n" \
  "ds_read_b64_tr_b16 v[78:79], v1 offset:3360\n" \
  "ds_read_b64_tr_b16 v[80:81], v1 offset:3328\n" \
  "s_waitcnt lgkmcnt(8)\n" \
  "v_mfma_f32_32x32x16_bf16 v[38:53], v[54:57], v[82:85], v[38:53]\n" \
  "ds_read_b128 v[102:105], v125 offset:512\n" \
  "s_waitcnt lgkmcnt(7)\n" \
  "v_mfma_f32_32x32x16_bf16 v[22:37], v[54:57], v[66:69], v[22:37]\n" \
  "ds_read_b64_tr_b16 v[110:111], v1 offset:3488\n" \
  "ds_read_b64_tr_b16 v[112:113], v1 offset:3456\n" \
  "s_waitcnt lgkmcnt(8)\n" \
  "v_mfma_f32_32x32x16_bf16 v[38:53], v[86:89], v[94:97], v[38:53]\n" \
  "ds_read_b128 v[106:109], v4 offset:384\n" \
  "s_waitcnt lgkmcnt(7)\n" \
  "v_mfma_f32_32x32x16_bf16 v[22:37], v[86:89], v[62:65], v[22:37]\n" \
  "ds_read_b64_tr_b16 v[90:91], v1 offset:3616\n" \
  "ds_read_b64_tr_b16 v[92:93], v1 offset:3584\n" \
  "s_waitcnt lgkmcnt(8)\n" \
  "v_mfma_f32_32x32x16_bf16 v[38:53], v[98:101], v[58:61], v[38:53]\n" \
  "ds_read_b128 v[74:77], v5 offset:256\n" \
  "s_waitcnt lgkmcnt(7)\n" \
  "v_mfma_f32_32x32x16_bf16 v[22:37], v[98:101], v[78:81], v[22:37]\n" \
  "ds_read_b64_tr_b16 v[82:83], v1 offset:3744\n" \
  "ds_read_b64_tr_b16 v[84:85], v1 offset:3712\n" \
  "s_waitcnt lgkmcnt(8)\n" \
  "v_mfma_f32_32x32x16_bf16 v[38:53], v[102:105], v[70:73], v[38:53]\n" \
  "ds_read_b128 v[54:57], v124 offset:128\n" \
  "s_waitcnt lgkmcnt(7)\n" \
  "v_mfma_f32_32x32x16_bf16 v[22:37], v[102:105], v[110:113], v[22:37]\n" \
  "ds_read_u8 v123, v3 offset:112\n" \
  "ds_read_b64_tr_b16 v[94:95], v1 offset:3872\n" \
  "ds_read_b64_tr_b16 v[96:97], v1 offset:3840\n" \
  "s_waitcnt lgkmcnt(9)\n" \
  "v_mfma_f32_32x32x16_bf16 v[38:53], v[106:109], v[66:69], v[38:53]\n" \
  "ds_read_b128 v[86:89], v125 offset:0\n" \
  "s_waitcnt lgkmcnt(8)\n" \
  "v_mfma_f32_32x32x16_bf16 v[22:37], v[106:109], v[90:93], v[22:37]\n" \
  "ds_read_b64_tr_b16 v[58:59], v1 offset:4000\n" \
  "ds_read_b64_tr_b16 v[60:61], v1 offset:3968\n" \
  "s_waitcnt lgkmcnt(9)\n" \
  "v_mfma_f32_32x32x16_bf16 v[38:53], v[74:77], v[62:65], v[38:53]\n" \
  "s_waitcnt lgkmcnt(5)\n" \
  "v_readfirstlane_b32 s20, v123\n" \
  "s_bitcmp1_b32 s20, 0\n" \
  "s_cbranch_scc0 .Lslow6_%=\n" \
  ".Lback6_%=:\n" \
  "ds_read_b128 v[98:101], v4 offset:3456\n" \
  "ds_read_b64_tr_b16 v[70:71], v1 offset:32\n" \
  "ds_read_b64_tr_b16 v[72:73], v1 offset:0\n" \
  "v_mfma_f32_32x32x16_bf16 v[22:37], v[74:77], v[82:85], v[22:37]\n" \
  "ds_read_b128 v[102:105], v5 offset:3328\n" \
  "ds_read_b64_tr_b16 v[66:67], v1 offset:160\n" \
  "ds_read_b64_tr_b16 v[68:69], v1 offset:128\n" \
  "v_mfma_f32_32x32x16_bf16 v[38:53], v[54:57], v[78:81], v[38:53]\n" \
  "ds_read_b128 v[106:109], v124 offset:3200\n" \
  "ds_read_b64_tr_b16 v[90:91], v1 offset:288\n" \
  "ds_read_b64_tr_b16 v[92:93], v1 offset:256\n" \
  "s_waitcnt lgkmcnt(12)\n" \
  "v_mfma_f32_32x32x16_bf16 v[22:37], v[54:57], v[94:97], v[22:37]\n" \
  "ds_read_b128 v[62:65], v125 offset:3072\n" \
  "ds_read_b64_tr_b16 v[74:75], v1 offset:416\n" \
  "ds_read_b64_tr_b16 v[76:77], v1 offset:384\n" \
  "s_waitcnt lgkmcnt(14)\n" \
  "v_mfma_f32_32x32x16_bf16 v[38:53], v[86:89], v[110:113], v[38:53]\n" \
  "ds_read_b64_tr_b16 v[82:83], v1 offset:544\n" \
  "ds_read_b64_tr_b16 v[84:85], v1 offset:512\n" \
  "s_waitcnt lgkmcnt(14)\n" \
  "v_mfma_f32_32x32x16_bf16 v[22:37], v[86:89], v[58:61], v[22:37]\n" \
  "ds_read_u8 v123, v3 offset:120\n" \
  "ds_read_b64_tr_b16 v[78:79], v1 offset:672\n" \
  "ds_read_b64_tr_b16 v[80:81], v1 offset:640\n" \
  "s_waitcnt lgkmcnt(14)\n" \
  "v_mfma_f32_32x32x16_bf16 v[38:53], v[98:101], v[70:73], v[38:53]\n" \
  "ds_read_b64_tr_b16 v[54:55], v1 offset:800\n" \
  "ds_read_b64_tr_b16 v[56:57], v1 offset:768\n" \
  "s_waitcnt lgkmcnt(13)\n" \
  "v_mfma_f32_32x32x16_bf16 v[38:53], v[102:105], v[66:69], v[38:53]\n" \
  "ds_read_b64_tr_b16 v[94:95], v1 offset:928\n" \
  "ds_read_b64_tr_b16 v[96:97], v1 offset:896\n" \
  "s_waitcnt lgkmcnt(12)\n" \
  "v_mfma_f32_32x32x16_bf16 v[38:53], v[106:109], v[90:93], v[38:53]\n" \
  "s_waitcnt lgkmcnt(6)\n" \
  "v_readfirstlane_b32 s20, v123\n" \
  "s_bitcmp1_b32 s20, 0\n" \
  "s_cbranch_scc0 .Lslow7_%=\n" \
  ".Lback7_%=:\n" \
  "ds_read_b128 v[110:113], v4 offset:3968\n" \
  "v_mfma_f32_32x32x16_bf16 v[38:53], v[62:65], v[74:77], v[38:53]\n" \
  "ds_read_b128 v[86:89], v5 offset:3840\n" \
  "s_setprio 0\n" \
  "v_mfma_f32_32x32x16_bf16 v[22:37], v[98:101], v[82:85], v[22:37]\n" \
  "ds_read_b128 v[58:61], v124 offset:3712\n" \
  "s_nop 7\n" \
  "s_waitcnt lgkmcnt(7)\n" \
  "v_mfma_f32_32x32x16_bf16 v[22:37], v[102:105], v[78:81], v[22:37]\n" \
  "ds_read_b128 v[98:101], v125 offset:3584\n" \
  "v_add_f32_dpp v114, v118, v40 wave_ror:1 row_mask:0xf bank_mask:0xf\n" \
  "v_add_f32_dpp v114, v41, v40 wave_shr:1 row_mask:0xf bank_mask:0xf\n" \
  "v_add_f32_dpp v115, v41, v44 wave_ror:1 row_mask:0xf bank_mask:0xf\n" \
  "v_add_f32_dpp v115, v45, v44 wave_shr:1 row_mask:0xf bank_mask:0xf\n" \
  "v_add_f32_dpp v116, v45, v48 wave_ror:1 row_mask:0xf bank_mask:0xf\n" \
  "s_waitcnt lgkmcnt(6)\n" \
  "v_mfma_f32_32x32x16_bf16 v[22:37], v[106:109], v[54:57], v[22:37]\n" \
  "v_add_f32_dpp v116, v49, v48 wave_shr:1 row_mask:0xf bank_mask:0xf\n" \
  "v_add_f32_dpp v119, v49, v52 wave_ror:1 row_mask:0xf bank_mask:0xf\n" \
  "v_add_f32_dpp v119, v53, v52 wave_shr:1 row_mask:0xf bank_mask:0xf\n" \
  "v_mov_b32 v118, v53\n" \
  "v_add_f32_dpp v40, v120, v39 wave_ror:1 row_mask:0xf bank_mask:0xf\n" \
  "v_add_f32_dpp v40, v114, v39 wave_shr:1 row_mask:0xf bank_mask:0xf\n" \
  "s_waitcnt lgkmcnt(4)\n" \
  "v_mfma_f32_32x32x16_bf16 v[22:37], v[62:65], v[94:97], v[22:37]\n" \
  "v_add_f32_dpp v44, v114, v43 wave_ror:1 row_mask:0xf bank_mask:0xf\n" \
  "v_add_f32_dpp v44, v115, v43 wave_shr:1 row_mask:0xf bank_mask:0xf\n" \
  "v_add_f32_dpp v48, v115, v47 wave_ror:1 row_mask:0xf bank_mask:0xf\n" \
  "v_add_f32_dpp v48, v116, v47 wave_shr:1 row_mask:0xf bank_mask:0xf\n" \
  "v_add_f32_dpp v121, v116, v51 wave_ror:1 row_mask:0xf bank_mask:0xf\n" \
  "v_add_f32_dpp v121, v119, v51 wave_shr:1 row_mask:0xf bank_mask:0xf\n" \
  "s_waitcnt lgkmcnt(3)\n" \
  "v_mfma_f32_32x32x16_bf16 v[22:37], v[110:113], v[70:73], v[22:37]\n" \
  "v_add_f32_dpp v114, v122, v38 wave_ror:1 row_mask:0xf bank_mask:0xf\n" \
  "v_add_f32_dpp v114, v40, v38 wave_shr:1 row_mask:0xf bank_mask:0xf\n" \
  "v_add_f32_dpp v115, v40, v42 wave_ror:1 row_mask:0xf bank_mask:0xf\n" \
  "v_add_f32_dpp v115, v44, v42 wave_shr:1 row_mask:0xf bank_mask:0xf\n" \
  "ds_write2st64_b32 v2, v114, v115 offset0:198 offset1:199\n" \
  "v_add_f32_dpp v116, v44, v46 wave_ror:1 row_mask:0xf bank_mask:0xf\n" \
  "s_waitcnt lgkmcnt(3)\n" \
  "v_mfma_f32_32x32x16_bf16 v[22:37], v[86:89], v[66:69], v[22:37]\n" \
  "v_add_f32_dpp v116, v48, v46 wave_shr:1 row_mask:0xf bank_mask:0xf\n" \
  "v_add_f32_dpp v117, v48, v50 wave_ror:1 row_mask:0xf bank_mask:0xf\n" \
  "v_add_f32_dpp v117, v121, v50 wave_shr:1 row_mask:0xf bank_mask:0xf\n" \
  "ds_write2st64_b32 v2, v116, v117 offset0:200 offset1:201\n" \
  "ds_write_b8 v3, v3 offset:48\n" \
  "s_waitcnt lgkmcnt(4)\n" \
  "v_mfma_f32_32x32x16_bf16 v[22:37], v[58:61], v[90:93], v[22:37]\n" \
  "s_waitcnt lgkmcnt(3)\n" \
  "v_mfma_f32_32x32x16_bf16 v[22:37], v[98:101], v[74:77], v[22:37]\n" \
  "s_nop 11\n" \
  "v_add_f32_dpp v114, v118, v24 wave_ror:1 row_mask:0xf bank_mask:0xf\n" \
  "v_add_f32_dpp v114, v25, v24 wave_shr:1 row_mask:0xf bank_mask:0xf\n" \
  "v_add_f32_dpp v115, v25, v28 wave_ror:1 row_mask:0xf bank_mask:0xf\n" \
  "v_add_f32_dpp v115, v29, v28 wave_shr:1 row_mask:0xf bank_mask:0xf\n" \
  "v_add_f32_dpp v116, v29, v32 wave_ror:1 row_mask:0xf bank_mask:0xf\n" \
  "v_add_f32_dpp v116, v33, v32 wave_shr:1 row_mask:0xf bank_mask:0xf\n" \
  "v_add_f32_dpp v120, v33, v36 wave_ror:1 row_mask:0xf bank_mask:0xf\n" \
  "v_add_f32_dpp v120, v37, v36 wave_shr:1 row_mask:0xf bank_mask:0xf\n" \
  "v_mov_b32 v118, v37\n" \
  "v_add_f32_dpp v24, v119, v23 wave_ror:1 row_mask:0xf bank_mask:0xf\n" \
  "v_add_f32_dpp v24, v114, v23 wave_shr:1 row_mask:0xf bank_mask:0xf\n" \
  "v_add_f32_dpp v28, v114, v27 wave_ror:1 row_mask:0xf bank_mask:0xf\n" \
  "v_add_f32_dpp v28, v115, v27 wave_shr:1 row_mask:0xf bank_mask:0xf\n" \
  "v_add_f32_dpp v32, v115, v31 wave_ror:1 row_mask:0xf bank_mask:0xf\n" \
  "v_add_f32_dpp v32, v116, v31 wave_shr:1 row_mask:0xf bank_mask:0xf\n" \
  "v_add_f32_dpp v122, v116, v35 wave_ror:1 row_mask:0xf bank_mask:0xf\n" \
  "v_add_f32_dpp v122, v120, v35 wave_shr:1 row_mask:0xf bank_mask:0xf\n" \
  "v_add_f32_dpp v114, v121, v22 wave_ror:1 row_mask:0xf bank_mask:0xf\n" \
  "v_add_f32_dpp v114, v24, v22 wave_shr:1 row_mask:0xf bank_mask:0xf\n" \
  "v_add_f32_dpp v115, v24, v26 wave_ror:1 row_mask:0xf bank_mask:0xf\n" \
  "v_add_f32_dpp v115, v28, v26 wave_shr:1 row_mask:0xf bank_mask:0xf\n" \
  "ds_write2st64_b32 v2, v114, v115 offset0:231 offset1:232\n" \
  "v_add_f32_dpp v116, v28, v30 wave_ror:1 row_mask:0xf bank_mask:0xf\n" \
  "v_add_f32_dpp v116, v32, v30 wave_shr:1 row_mask:0xf bank_mask:0xf\n" \
  "v_add_f32_dpp v117, v32, v34 wave_ror:1 row_mask:0xf bank_mask:0xf\n" \
  "v_add_f32_dpp v117, v122, v34 wave_shr:1 row_mask:0xf bank_mask:0xf\n" \
  "ds_write2st64_b32 v2, v116, v117 offset0:233 offset1:234\n" \
  "ds_write_b8 v3, v3 offset:56\n" \
  "s_branch .Lend_%=\n" \
  ".Lslow8_%=:\n" \
  "ds_read_u8 v123, v3 offset:128\n" \
  "s_waitcnt lgkmcnt(0)\n" \
  "v_readfirstlane_b32 s20, v123\n" \
  "s_bitcmp1_b32 s20, 0\n" \
  "s_cbranch_scc1 .Lback8_%=\n" \
  "s_branch .Lslow8_%=\n" \
  ".Lslow0_%=:\n" \
  "ds_read_u8 v123, v3 offset:64\n" \
  "s_waitcnt lgkmcnt(0)\n" \
  "v_readfirstlane_b32 s20, v123\n" \
  "s_bitcmp1_b32 s20, 0\n" \
  "s_cbranch_scc1 .Lback0_%=\n" \
  "s_branch .Lslow0_%=\n" \
  ".Lslow1_%=:\n" \
  "ds_read_u8 v123, v3 offset:72\n" \
  "s_waitcnt lgkmcnt(0)\n" \
  "v_readfirstlane_b32 s20, v123\n" \
  "s_bitcmp1_b32 s20, 0\n" \
  "s_cbranch_scc1 .Lback1_%=\n" \
  "s_branch .Lslow1_%=\n" \
  ".Lslow2_%=:\n" \
  "ds_read_u8 v123, v3 offset:80\n" \
  "s_waitcnt lgkmcnt(0)\n" \
  "v_readfirstlane_b32 s20, v123\n" \
  "s_bitcmp1_b32 s20, 0\n" \
  "s_cbranch_scc1 .Lback2_%=\n" \
  "s_branch .Lslow2_%=\n" \
  ".Lslow3_%=:\n" \
  "ds_read_u8 v123, v3 offset:88\n" \
  "s_waitcnt lgkmcnt(0)\n" \
  "v_readfirstlane_b32 s20, v123\n" \
  "s_bitcmp1_b32 s20, 0\n" \
  "s_cbranch_scc1 .Lback3_%=\n" \
  "s_branch .Lslow3_%=\n" \
  ".Lslow9_%=:\n" \
  "ds_read_u8 v123, v3 offset:136\n" \
  "s_waitcnt lgkmcnt(0)\n" \
  "v_readfirstlane_b32 s20, v123\n" \
  "s_bitcmp1_b32 s20, 0\n" \
  "s_cbranch_scc1 .Lback9_%=\n" \
  "s_sleep 1\n" \
  "s_branch .Lslow9_%=\n" \
  ".Lslow4_%=:\n" \
  "ds_read_u8 v123, v3 offset:96\n" \
  "s_waitcnt lgkmcnt(0)\n" \
  "v_readfirstlane_b32 s20, v123\n" \
  "s_bitcmp1_b32 s20, 0\n" \
  "s_cbranch_scc1 .Lback4_%=\n" \
  "s_sleep 1\n" \
  "s_branch .Lslow4_%=\n" \
  ".Lslow5_%=:\n" \
  "ds_read_u8 v123, v3 offset:104\n" \
  "s_waitcnt lgkmcnt(0)\n" \
  "v_readfirstlane_b32 s20, v123\n" \
  "s_bitcmp1_b32 s20, 0\n" \
  "s_cbranch_scc1 .Lback5_%=\n" \
  "s_sleep 1\n" \
  "s_branch .Lslow5_%=\n" \
  ".Lslow6_%=:\n" \
  "ds_read_u8 v123, v3 offset:112\n" \
  "s_waitcnt lgkmcnt(0)\n" \
  "v_readfirstlane_b32 s20, v123\n" \
  "s_bitcmp1_b32 s20, 0\n" \
  "s_cbranch_scc1 .Lback6_%=\n" \
  "s_sleep 1\n" \
  "s_branch .Lslow6_%=\n" \
  ".Lslow7_%=:\n" \
  "ds_read_u8 v123, v3 offset:120\n" \
  "s_waitcnt lgkmcnt(0)\n" \
  "v_readfirstlane_b32 s20, v123\n" \
  "s_bitcmp1_b32 s20, 0\n" \
  "s_cbranch_scc1 .Lback7_%=\n" \
  "s_sleep 1\n" \
  "s_branch .Lslow7_%=\n" \
  ".Lend_%=:\n" \
  "s_waitcnt lgkmcnt(0)\n"
#define MFMA_CLOBBERS "v1", "v2", "v3", "v4", "v5", "v6", "v7", "v8", "v9", "v10", "v11", "v12", "v13", "v14", "v15", "v16", "v17", "v18", "v19", "v20", "v21", "v22", "v23", "v24", "v25", "v26", "v27", "v28", "v29", "v30", "v31", "v32", "v33", "v34", "v35", "v36", "v37", "v38", "v39", "v40", "v41", "v42", "v43", "v44", "v45", "v46", "v47", "v48", "v49", "v50", "v51", "v52", "v53", "v54", "v55", "v56", "v57", "v58", "v59", "v60", "v61", "v62", "v63", "v64", "v65", "v66", "v67", "v68", "v69", "v70", "v71", "v72", "v73", "v74", "v75", "v76", "v77", "v78", "v79", "v80", "v81", "v82", "v83", "v84", "v85", "v86", "v87", "v88", "v89", "v90", "v91", "v92", "v93", "v94", "v95", "v96", "v97", "v98", "v99", "v100", "v101", "v102", "v103", "v104", "v105", "v106", "v107", "v108", "v109", "v110", "v111", "v112", "v113", "v114", "v115", "v116", "v117", "v118", "v119", "v120", "v121", "v122", "v123", "v124", "v125", "s20", "scc", "memory"

typedef unsigned short u16;
typedef unsigned long long u64;
typedef __attribute__((ext_vector_type(4))) unsigned u32x4;
typedef __attribute__((ext_vector_type(2))) unsigned u32x2;
typedef __attribute__((ext_vector_type(4))) u64 u64x4;
typedef __attribute__((ext_vector_type(2))) u64 u64x2;
typedef __attribute__((ext_vector_type(2))) float f32x2;
typedef __attribute__((ext_vector_type(4))) float f32x4;
typedef __attribute__((ext_vector_type(2))) __bf16 bf16x2;
typedef __attribute__((ext_vector_type(8))) __bf16 bf16x8;
#if defined(__HIP_DEVICE_COMPILE__)
#pragma clang attribute push(__attribute__((target("no-packed-fp32-ops"))), apply_to = function)
#endif
#define LAS __attribute__((address_space(3)))
#define CAS __attribute__((address_space(4)))
#define GAS __attribute__((address_space(1)))

constexpr int TBYTES = 2048 * 1280;
constexpr int HSTR = 4160;
constexpr int XOFF = 8 * HSTR;
constexpr int XSTR = 4544;
constexpr int YOFF = XOFF + 8 * XSTR + 32;
constexpr int YSTR = 8448;
constexpr int FOFF = YOFF + 8 * YSTR;
constexpr int LDS_BYTES = FOFF + 1024;
static_assert(((FOFF + 16) & 0x1f) == 0x10, "y-ready bytes carry bit 4 of their own address");
constexpr int NHELP = 12;
constexpr int NT = 4;
constexpr int FS = 128;
constexpr int WS_REC = 262144;
constexpr int REC_O = NT * 4096;
constexpr int REC_BYTES = 160 * REC_O;
constexpr u64 TAG_STEP = 0x9E3779B97F4A7C15ull;
constexpr int SPIN_CAP = 1 << 17;
constexpr unsigned OOB = 0x80000000u;
constexpr int PACE = 5, QDELAY = 350, POLL0 = 430, POLLT = 80, POLLS = 3;
constexpr int THLO = 3;
constexpr int HHI = 3;
constexpr int CTN = 2, CT2 = 31;
constexpr int AUX_NT = 2, AUX_SC1 = 16;

__device__ __forceinline__ unsigned pk(float a, float b) {
  bf16x2 p = __builtin_convertvector(f32x2{a, b}, bf16x2);
  return __builtin_bit_cast(unsigned, p);
}
__device__ __forceinline__ float lo16(unsigned d) { return __builtin_bit_cast(float, d << 16); }
__device__ __forceinline__ float hi16(unsigned d) { return __builtin_bit_cast(float, d & 0xffff0000u); }
__device__ __forceinline__ void wait_tok(LAS char* p, unsigned t) {
  while ((unsigned)__builtin_amdgcn_readfirstlane(__hip_atomic_load((LAS unsigned*)p, __ATOMIC_RELAXED, __HIP_MEMORY_SCOPE_WORKGROUP)) < t)
    __builtin_amdgcn_s_sleep(1);
  asm volatile("" ::: "memory");
}
__device__ __forceinline__ void wait_y(LAS char* p) {
  for (;;) {
    const u64 v = __hip_atomic_load((LAS u64*)p, __ATOMIC_RELAXED, __HIP_MEMORY_SCOPE_WORKGROUP);
    if (__builtin_amdgcn_readfirstlane((unsigned)v & (unsigned)(v >> 32) & 0x10101010u) == 0x10101010u) break;
    __builtin_amdgcn_s_sleep(1);
  }
  asm volatile("" ::: "memory");
}
__device__ __forceinline__ u64 gpoll(u64 p) {
  u64 z = 0;
  asm volatile("" : "+v"(z));
  return __hip_atomic_fetch_add((GAS u64*)p, z, __ATOMIC_RELAXED, __HIP_MEMORY_SCOPE_AGENT);
}
__device__ __forceinline__ void gst(u64 p, u64 v) { __hip_atomic_store((GAS u64*)p, v, __ATOMIC_RELAXED, __HIP_MEMORY_SCOPE_AGENT); }
__device__ __forceinline__ u64 call_tag(u64 ctl) {
  u64 e;
  asm volatile("s_load_dwordx2 %0, %1, 0x0\n\ts_waitcnt lgkmcnt(0)" : "=s"(e) : "s"(ctl) : "memory");
  return e + TAG_STEP;
}
#define RSRC(p, n) __builtin_amdgcn_make_buffer_rsrc((void*)(p), 0, n, 0x00020000)

struct Args { const u16* in[12]; u16* out; char* ws; };

__global__ __launch_bounds__(1024, 4) void h3_kernel(Args args_) {
  extern __shared__ __attribute__((aligned(32))) char smem_[];
  LAS char* smem = (LAS char*)smem_;
  const int tid = __builtin_amdgcn_workitem_id_x();
  const int wv = __builtin_amdgcn_readfirstlane(tid >> 6), lane = tid & 63;
  const int b = __builtin_amdgcn_workgroup_id_x(), xg = b & 7, half = xg >> 2;
  const bool helper = b < 8 * NHELP;
  const CAS u64* ka = (const CAS u64*)__builtin_amdgcn_kernarg_segment_ptr();
  u64x4 pa; u64x2 pb, pc2; u64 tstart;
  asm volatile("s_load_dwordx8 %0, %4, %5\n\ts_load_dwordx4 %1, %4, %5 offset:0x20\n\ts_load_dwordx4 %2, %4, 0x60\n\ts_memrealtime %3"
               : "=&s"(pa), "=&s"(pb), "=&s"(pc2), "=&s"(tstart) : "s"(ka), "s"(half * 48));
#define ARGS_READY() asm volatile("s_waitcnt lgkmcnt(0)" : "+s"(pa), "+s"(pb), "+s"(pc2), "+s"(tstart))
#define pq pa[0]
#define pkk pa[1]
#define pv pa[2]
#define ps pa[3]
#define ph pb[0]
#define pd pb[1]
#define po pc2[0]
#define pw pc2[1]

  asm volatile(
      "s_cmp_lt_u32 %[b], %[nh]\n\t"
      "s_cbranch_scc1 .Lhelp_%=\n\t"
      "s_cmp_lt_u32 %[wv], 8\n\t"
      "s_cbranch_scc1 .Lend_%=\n\t"
      "s_barrier\n\t"
      "s_branch .Lend_%=\n"
      ".Lhelp_%=:\n\t"
      "s_mov_b32 m0, 0\n\t"
      "s_bfe_u32 s40, %[b], 0x10002\n\t"
      "s_mul_i32 s40, s40, 48\n\t"
      "s_lshl_b32 s41, %[wv], 1\n\t"
      "s_lshr_b32 s42, 0x555555, s41\n\t"
      "s_and_b32 s42, s42, 1\n\t"
      "s_lshl_b32 s43, s42, 1\n\t"
      "s_bitcmp1_b32 0x66, %[wv]\n\t"
      "s_cselect_b32 s42, 4, s42\n\t"
      "s_cselect_b32 s43, 4, s43\n\t"
      "s_lshl3_add_u32 s42, s42, s40\n\t"
      "s_lshl3_add_u32 s43, s43, s40\n\t"
      "s_load_dwordx2 s[44:45], %[ka], s42\n\t"
      "s_load_dwordx2 s[48:49], %[ka], s43\n\t"
      "s_lshl_b32 s41, %[wv], 8\n\t"
      "s_add_u32 s41, s41, %[b]\n\t"
      "s_cmp_ge_u32 s41, %[ctn]\n\t"
      "s_cbranch_scc1 .Lnc_%=\n\t"
      "s_getpc_b64 s[52:53]\n\t"
      "v_lshlrev_b32 v1, 7, %[ln]\n\t"
      "global_load_lds_dword v1, s[52:53]\n\t"
      "v_min_u32 v1, %[ct2], v1\n\t"
      "v_add_u32 v1, 0x2000, v1\n\t"
      "global_load_lds_dword v1, s[52:53]\n"
      ".Lnc_%=:\n\t"
      "s_lshr_b32 s40, %[b], 3\n\t"
      "s_and_b32 s41, %[b], 3\n\t"
      "s_mov_b32 s47, 0x20000\n\t"
      "s_bitcmp1_b32 0x66, %[wv]\n\t"
      "s_cbranch_scc0 .Lrow_%=\n\t"
      "s_bitcmp1_b32 %[wv], 0\n\t"
      "s_cselect_b32 s42, 0, 24\n\t"
      "s_add_u32 s42, s42, s40\n\t"
      "s_lshl_b32 s42, s42, 14\n\t"
      "s_and_b32 s43, %[wv], 4\n\t"
      "s_lshl_b32 s43, s43, 9\n\t"
      "s_add_u32 s42, s42, s43\n\t"
      "s_mul_i32 s41, s41, 0xa0000\n\t"
      "s_add_u32 s42, s42, s41\n\t"
      "s_add_u32 s46, s41, 0xa0000\n\t"
      "s_add_u32 s43, s42, 0x30000\n\t"
      "s_mov_b32 s40, 0\n\t"
      "s_movk_i32 s52, 0x1000\n\t"
      "s_movk_i32 s53, 0x800\n\t"
      "s_movk_i32 s54, 0x80\n\t"
      "s_branch .Lgo_%=\n"
      ".Lrow_%=:\n\t"
      "s_lshl_b32 s42, %[wv], 2\n\t"
      "s_lshr_b64 s[52:53], %[blk], s42\n\t"
      "s_and_b32 s42, s52, 15\n\t"
      "s_mul_i32 s42, s42, 0x50000\n\t"
      "s_mul_i32 s41, s41, 320\n\t"
      "s_add_u32 s42, s42, s41\n\t"
      "s_mov_b32 s46, 0x280000\n\t"
      "s_cmp_ge_u32 %[wv], 12\n\t"
      "s_cselect_b32 s43, 0x140000, 0\n\t"
      "s_add_u32 s43, s43, s42\n\t"
      "s_movk_i32 s52, 21846\n\t"
      "s_movk_i32 s53, 812\n\t"
      "s_movk_i32 s54, 156\n"
      ".Lgo_%=:\n\t"
      "v_lshl_add_u32 v1, s40, 6, %[ln]\n\t"
      "v_mul_u32_u24 v2, s52, v1\n\t"
      "v_mul_u32_u24 v1, s54, v1\n\t"
      "v_lshrrev_b32 v2, 16, v2\n\t"
      "v_mad_u32_u24 v1, v2, s53, v1\n\t"
      "s_mov_b32 s50, s46\n\t"
      "s_mov_b32 s51, s47\n\t"
      "v_add_u32 v2, s43, v1\n\t"
      "v_add_u32 v1, s42, v1\n\t"
      "s_mov_b32 s40, %[wv]\n"
      ".Lsl_%=:\n\t"
      "s_sub_u32 s40, s40, 1\n\t"
      "s_cbranch_scc1 .Lld_%=\n\t"
      "s_sleep %[pace]\n\t"
      "s_branch .Lsl_%=\n"
      ".Lld_%=:\n\t"
      "s_waitcnt lgkmcnt(0)\n\t"
      "s_and_b32 s45, s45, 0xffff\n\t"
      "s_and_b32 s49, s49, 0xffff\n\t"
      "buffer_load_dword v1, s[44:47], 0 offen lds\n\t"
      "buffer_load_dword v2, s[48:51], 0 offen lds\n"
      ".Lend_%=:"
      :: [b] "s"(b), [wv] "s"(wv), [ka] "s"(ka), [ln] "v"(lane), [nh] "i"(8 * NHELP), [ctn] "i"(8 * CTN), [ct2] "i"((CT2 - 1) * 128),
         [pace] "i"(PACE), [blk] "s"(0x765430021000ull + ((u64)(NT * 0x1111 + 0x3210) << 48))
      : "s40", "s41", "s42", "s43", "s44", "s45", "s46", "s47", "s48", "s49", "s50", "s51", "s52", "s53", "s54", "v1", "v2", "scc", "memory");

  if (__builtin_expect(!helper, 1)) {
    LAS char* flags = smem + FOFF;
    const int ol = (xg & 3) * 20 + (b >> 3) - NHELP;
    if (__builtin_expect(wv < 8, 1)) {
      __builtin_amdgcn_s_setprio(3);
      const int dl = wv;
      const bool hdma = dl == 1 || dl == HHI;
      const int col = lane & 15, g = lane >> 4;
      const int row0 = dl * 256 + col * 16 + g;
      const unsigned voff = row0 * 1280 + ol * 16;
      const unsigned hoff = (col == 0 && dl > 0 && g > 0) ? voff - 5120 : OOB;
      const int tap = ((lane >> 2) - g) & 3;
      const unsigned woff = (ol * 8 + (lane & 3)) * 8 + (tap >> 1) * 4;
      LAS char* f1 = flags + (lane ? 256 + lane * 8 : 0);
      f32x4 e[4][2];
      asm volatile("" :: "v"(voff), "v"(hoff), "v"(woff));
      ARGS_READY();
      if (dl != 0) {
        __builtin_amdgcn_s_barrier();
        if (dl > 1) wait_tok(flags + 12, dl);
        if (hdma) {
          u64 hg = ph + (u64)ol * 32768 + (dl > 1 ? 2048 : 0);
          unsigned hm = (unsigned)(size_t)(smem + 64 + (dl > 1 ? 2048 : 0)), hv = lane * 16;
          asm volatile("s_mov_b32 m0, %1\n"
                       ".Lh_%=:\n\t"
                       "s_add_u32 %1, %1, %3\n\t"
                       "global_load_lds_dwordx4 %2, %0\n\t"
                       "global_load_lds_dwordx4 %2, %0 offset:1024\n\t"
                       "v_add_u32 %2, 0x1000, %2\n\t"
                       "s_mov_b32 m0, %1\n\t"
                       "s_cmp_lt_u32 %1, %4\n\t"
                       "s_cbranch_scc1 .Lh_%="
                       : "+s"(hg), "+s"(hm), "+v"(hv) : "i"(HSTR), "i"(8 * HSTR) : "scc", "memory");
        }
      }
      const auto rs = RSRC(ps, 640 * 8);
      const auto rk = RSRC(pkk, TBYTES);
      const auto rv = RSRC(pv, TBYTES);
      u32x4 kk[5], vv[4];
      const unsigned wd0 = __builtin_amdgcn_raw_buffer_load_b32(rs, woff, 0, 0), wd1 = __builtin_amdgcn_raw_buffer_load_b32(rs, woff + 32, 0, 0);
      kk[0] = __builtin_amdgcn_raw_buffer_load_b128(rk, hoff, 0, 0);
#pragma unroll
      for (int i = 0; i < 4; ++i) kk[1 + i] = __builtin_amdgcn_raw_buffer_load_b128(rk, voff + i * 5120, 0, 0);
#pragma unroll
      for (int i = 0; i < 4; ++i) vv[i] = __builtin_amdgcn_raw_buffer_load_b128(rv, voff + i * 5120, 0, 0);
      __builtin_amdgcn_sched_barrier(0);
      if (dl == 0) {
        if (lane < 10) *(LAS u32x4*)(flags + lane * 16) = u32x4{0, 0, 0, 0};
        asm volatile("s_waitcnt lgkmcnt(0)" ::: "memory");
        __builtin_amdgcn_s_barrier();
      } else {
        if (dl >= THLO) asm volatile("s_waitcnt vmcnt(7)" ::: "memory");
        *(volatile LAS unsigned*)(f1 + 12) = dl + 1;
        if (hdma) {
          asm volatile("s_waitcnt vmcnt(9)" ::: "memory");
          *(volatile LAS u64*)(f1 + (dl > 1 ? 152 : 144)) = 0x0101010101010101ull;
        }
      }
      __builtin_amdgcn_sched_barrier(0);
      const bool cur = g <= ((lane >> 2) & 3), hi2 = lane & 2;
      const unsigned s0 = ((wd0 >> ((tap & 1) * 16)) & 0xffffu) << ((lane & 1) * 16), s1 = ((wd1 >> ((tap & 1) * 16)) & 0xffffu) << ((lane & 1) * 16);
      const unsigned a0l = hi2 ? 0u : s0, a0h = hi2 ? s0 : 0u, a1l = hi2 ? 0u : s1, a1h = hi2 ? s1 : 0u;
      const bf16x8 A0 = __builtin_bit_cast(bf16x8, u32x4{cur ? 0u : a0l, cur ? 0u : a0h, cur ? a0l : 0u, cur ? a0h : 0u});
      const bf16x8 A1 = __builtin_bit_cast(bf16x8, u32x4{cur ? 0u : a1l, cur ? 0u : a1h, cur ? a1l : 0u, cur ? a1h : 0u});
      asm volatile("" :: "v"(A0), "v"(A1));
      __builtin_amdgcn_sched_barrier(0);
#pragma unroll
      for (int d = 0; d < 4; ++d) kk[0][d] = __builtin_amdgcn_update_dpp(kk[0][d], kk[4][d], 0x111, 0xf, 0xf, false);
#pragma unroll
      for (int ii = 0; ii < 4; ++ii) {
        const int i = (ii + 1) & 3;
        e[i][0] = __builtin_amdgcn_mfma_f32_16x16x32_bf16(A0, __builtin_bit_cast(bf16x8, u32x4{kk[i].x, kk[i].y, kk[i + 1].x, kk[i + 1].y}), f32x4{}, 0, 0, 0);
        e[i][1] = __builtin_amdgcn_mfma_f32_16x16x32_bf16(A1, __builtin_bit_cast(bf16x8, u32x4{kk[i].z, kk[i].w, kk[i + 1].z, kk[i + 1].w}), f32x4{}, 0, 0, 0);
      }
      __builtin_amdgcn_sched_barrier(0);
#pragma unroll
      for (int i = 0; i < 4; ++i)
#pragma unroll
        for (int d = 0; d < 4; ++d) {
          e[i][d >> 1][(d & 1) * 2] *= lo16(vv[i][d]);
          e[i][d >> 1][(d & 1) * 2 + 1] *= hi16(vv[i][d]);
        }
      LAS char* xw = smem + XOFF + 448 + (dl * 8 + (col >> 1)) * 64 + (g ^ (col >> 2)) * 16 + (col & 1) * 8;
#pragma unroll
      for (int cc = 0; cc < 8; ++cc)
        *(LAS u32x2*)(xw + cc * XSTR + (cc >> 2) * 32) =
            u32x2{pk(e[0][cc >> 2][cc & 3], e[1][cc >> 2][cc & 3]), pk(e[2][cc >> 2][cc & 3], e[3][cc >> 2][cc & 3])};
      asm volatile("" ::: "memory");
      *(volatile LAS u64*)(f1 + 80 + 8 * dl) = 0x0101010101010101ull;
      asm volatile("" ::: "memory");
      const u32x4 dr = *(const CAS u32x4*)(pd + ol * 16);
      float dd[8];
#pragma unroll
      for (int d = 0; d < 4; ++d) { dd[2 * d] = lo16(dr[d]); dd[2 * d + 1] = hi16(dr[d]); }
      LAS char* yr = smem + YOFF + dl * YSTR + col * 64 + g * 16;
      f32x4 y[8];
      if (dl < NT) {
        const int og = half * 80 + ol;
        const u64 ctl = pw + (og >> 3) * 8192, tag = call_tag(ctl);
        wait_y(flags + 16 + 8 * dl);
#pragma unroll
        for (int cc = 0; cc < 8; ++cc) y[cc] = *(LAS f32x4*)(yr + cc * 1024);
        const auto rz = RSRC(pw + WS_REC, REC_BYTES);
#pragma unroll
        for (int i = 0; i < 4; ++i) {
          float o[8];
#pragma unroll
          for (int cc = 0; cc < 8; ++cc) o[cc] = __builtin_fmaf(e[i][cc >> 2][cc & 3], dd[cc], y[cc][i]);
          __builtin_amdgcn_raw_buffer_store_b128(u32x4{pk(o[0], o[1]), pk(o[2], o[3]), pk(o[4], o[5]), pk(o[6], o[7])}, rz,
                                                 og * REC_O + dl * 4096 + i * 1024 + lane * 16, 0, AUX_SC1);
        }
        asm volatile("s_waitcnt vmcnt(0)" ::: "memory");
        if (lane == 0) gst(ctl + 1024 + (dl * 8 + (og & 7)) * FS, tag);
      } else {
        const auto rq = RSRC(pq, TBYTES);
        u32x4 qq[4];
        wait_tok(flags + 12, 8 + dl - NT);
#pragma unroll
        for (int i = 0; i < 4; ++i) qq[i] = __builtin_amdgcn_raw_buffer_load_b128(rq, voff + i * 5120, 0, 0);
        __builtin_amdgcn_sched_barrier(0);
        *(volatile LAS unsigned*)(f1 + 12) = 9 + dl - NT;
        __builtin_amdgcn_sched_barrier(0);
        float qf[4][8];
#pragma unroll
        for (int i = 0; i < 4; ++i)
#pragma unroll
          for (int d = 0; d < 4; ++d) {
            qf[i][2 * d] = lo16(qq[i][d]); qf[i][2 * d + 1] = hi16(qq[i][d]);
            asm volatile("" : "+v"(qf[i][2 * d]), "+v"(qf[i][2 * d + 1]));
          }
        wait_y(flags + 16 + 8 * dl);
#pragma unroll
        for (int cc = 0; cc < 8; ++cc) y[cc] = *(LAS f32x4*)(yr + cc * 1024);
        const auto ro = RSRC(po, 2 * TBYTES);
        const unsigned ooff = row0 * 2560 + half * 1280 + ol * 16;
#pragma unroll
        for (int i = 0; i < 4; ++i) {
          float o[8];
#pragma unroll
          for (int cc = 0; cc < 8; ++cc) o[cc] = qf[i][cc] * __builtin_fmaf(e[i][cc >> 2][cc & 3], dd[cc], y[cc][i]);
          const u32x4 ov = u32x4{pk(o[0], o[1]), pk(o[2], o[3]), pk(o[4], o[5]), pk(o[6], o[7])};
          __builtin_amdgcn_raw_buffer_store_b128(ov, ro, ooff + i * 10240, 0, 0);
          __builtin_amdgcn_sched_barrier(0);
        }
      }
    } else {
      ARGS_READY();
      const int c = wv - 8;
      LAS char* hb = smem + c * HSTR;
      LAS char* xb = smem + XOFF + c * XSTR + (c >> 2) * 32;
      if (lane < 32) *(LAS u32x4*)(lane < 28 ? xb + lane * 16 : hb + (lane - 28) * 16) = u32x4{0, 0, 0, 0};
      asm volatile(MFMA_PROGRAM
                   :
                   : [ln] "v"(lane), [hb] "s"(hb + 32), [xb] "s"(xb + 64), [yb] "s"(smem + YOFF + c * 1024), [fb] "s"(flags + 16 + c)
                   : MFMA_CLOBBERS);
      __builtin_amdgcn_endpgm();
    }
  } else {
    ARGS_READY();
    const int id = wv * NHELP + (b >> 3), tile = id / 20, un = xg * 20 + id - tile * 20, cb = un >> 3, w8 = un & 7;
    const u64 ctl = pw + cb * 8192;
    if (tile < NT) {
      const u64 tag = call_tag(ctl);
      const int o = lane & 7, r = lane >> 3;
      const int row = tile * 256 + r * 16 + w8 * 2;
      const auto r1 = RSRC(pq, TBYTES);
      while (__builtin_amdgcn_s_memrealtime() - tstart < (u64)QDELAY) __builtin_amdgcn_s_sleep(4);
      if (lane == 0) gst(ctl + 256 + tile * 64 + w8 * 8, tag);
      u32x4 qq[4], zz[4];
#pragma unroll
      for (int n = 0; n < 4; ++n)
        qq[n] = __builtin_amdgcn_raw_buffer_load_b128(r1, (row + (n & 1) * 128 + (n >> 1)) * 1280 + (cb - half * 10) * 128 + o * 16, 0, AUX_NT);
      while (__builtin_amdgcn_s_memrealtime() - tstart < (u64)(POLL0 + tile * POLLT)) __builtin_amdgcn_s_sleep(4);
#pragma unroll 1
      for (int spin = 0; spin < SPIN_CAP; ++spin) {
        u64 fv = tag;
        if (lane < 8) fv = gpoll(ctl + 1024 + (tile * 8 + o) * FS);
        if (__builtin_amdgcn_ballot_w64(fv == tag) == ~0ull) break;
        __builtin_amdgcn_s_sleep(POLLS);
      }
      asm volatile("" ::: "memory");
      const auto rz = RSRC(pw + WS_REC, REC_BYTES);
#pragma unroll
      for (int n = 0; n < 4; ++n)
        zz[n] = __builtin_amdgcn_raw_buffer_load_b128(rz, (cb * 8 + o) * REC_O + tile * 4096 + (w8 * 32 + n * 8 + r) * 16, 0, AUX_SC1);
      const auto ro = RSRC(po, 2 * TBYTES);
#pragma unroll
      for (int n = 0; n < 4; ++n) {
        u32x4 ov;
#pragma unroll
        for (int d = 0; d < 4; ++d) ov[d] = pk(lo16(qq[n][d]) * lo16(zz[n][d]), hi16(qq[n][d]) * hi16(zz[n][d]));
        __builtin_amdgcn_raw_buffer_store_b128(ov, ro, (row + (n & 1) * 128 + (n >> 1)) * 2560 + cb * 128 + o * 16, 0, AUX_SC1);
      }
    } else if (tile == NT && w8 == 0) {
      const u64 tag = call_tag(ctl);
      const int t = (lane >> 3) < NT ? (lane >> 3) : 0;
      while (__builtin_amdgcn_s_memrealtime() - tstart < (u64)(POLL0 + (NT - 1) * POLLT + 20)) __builtin_amdgcn_s_sleep(4);
#pragma unroll 1
      for (int spin = 0; spin < SPIN_CAP; ++spin) {
        const u64 f0 = gpoll(ctl + 1024 + (t * 8 + (lane & 7)) * FS), f1 = gpoll(ctl + 256 + t * 64 + (lane & 7) * 8);
        if (__builtin_amdgcn_ballot_w64((f0 == tag) & (f1 == tag)) == ~0ull) break;
        __builtin_amdgcn_s_sleep(2 * POLLS);
      }
      if (lane == 0) gst(ctl, tag);
    }
    asm volatile("s_waitcnt vmcnt(0)" ::: "memory");
  }
}

#if defined(__HIP_DEVICE_COMPILE__)
#pragma clang attribute pop
#endif
extern "C" void kernel_launch(void* const* d_in, const int* in_sizes, int n_in, void* d_out, int out_size,
                              void* d_ws, size_t ws_size, hipStream_t stream) {
  Args a;
  for (int i = 0; i < 12; ++i) a.in[i] = (const u16*)d_in[i];
  a.out = (u16*)d_out;
  a.ws = (char*)d_ws;
  (void)hipFuncSetAttribute((const void*)h3_kernel, hipFuncAttributeMaxDynamicSharedMemorySize, LDS_BYTES);
  h3_kernel<<<8 * (20 + NHELP), 1024, LDS_BYTES, stream>>>(a);
}
```
